```python
import torch

def _softmax(x, axis=-1):
    return torch.softmax(x, dim=axis)
def _gelu(x):
    return torch.nn.functional.gelu(x, approximate="tanh")
def _silu(x):
    return torch.nn.functional.silu(x)
def _sigmoid(x):
    return torch.sigmoid(x)
def _layernorm(x, axis=-1, eps=1e-5):
    m = x.mean(dim=axis, keepdim=True)
    v = x.var(dim=axis, keepdim=True, unbiased=False)
    return (x - m) / torch.sqrt(v + eps)
def _scatter_add(tgt, idx, src, axis=0):
    return tgt.clone().index_add_(axis, idx, src)
def _segment_sum(src, seg, n):
    out = torch.zeros((n,) + src.shape[1:], dtype=src.dtype)
    return out.index_add_(0, seg, src)
def _pad(x, pw):
    flat = tuple(v for pair in reversed(pw) for v in pair)
    return torch.nn.functional.pad(x, flat)

kernel_name = 'standalone__paged_block_table_kv_pagedattention__decode'

def setup_inputs():

    _w = (lambda a: (lambda t: t.to(torch.bfloat16) if t.is_floating_point() else t)(torch.as_tensor(a)))
    B, Hq, Hkv, Sq, Skv, D = 2, 32, 4, 1, 32768, 128
    bs = 16
    nbps = max(1, Skv // bs)
    num_phys = B * nbps + 4
    bt = torch.randperm(num_phys)[: B * nbps].reshape(B, nbps).to(torch.int64)
    cl = (nbps * bs - (torch.arange(1, B + 1, dtype=torch.int64) * 2654435761) % max(1, nbps * bs // 2)).to(torch.int64)
    return {
        "q": _w((lambda *s: torch.randn(*s, dtype=torch.bfloat16) * 0.8)(B, Hq, Sq, D)),
        "k_cache": _w((lambda *s: torch.randn(*s, dtype=torch.bfloat16) * 0.8)(num_phys, bs, Hkv, D)),
        "v_cache": _w((lambda *s: torch.randn(*s, dtype=torch.bfloat16) * 0.05)(num_phys, bs, Hkv, D)),
        "block_table": _w(bt),
        "context_lens": _w(cl),
    }

def reference(q, k_cache, v_cache, block_table, context_lens):
    B, Hq, Sq, D = q.shape
    bs = k_cache.shape[1]
    Hkv = k_cache.shape[2]
    nbps = block_table.shape[1]
    Skv = nbps * bs
    bt = block_table.reshape(-1)
    k = (lambda x, *d: x.permute(*d))(k_cache[bt].reshape(B, Skv, Hkv, D), 0, 2, 1, 3)
    v = (lambda x, *d: x.permute(*d))(v_cache[bt].reshape(B, Skv, Hkv, D), 0, 2, 1, 3)
    g = Hq // Hkv
    qg = q.reshape(B, Hkv, g, Sq, D)
    scores = torch.einsum("bhgqd,bhkd->bhgqk", qg, k) * (D ** -0.5)
    pos = torch.arange(Skv)
    msk = pos[None, :] < context_lens[:, None]
    scores = torch.where(msk[:, None, None, None, :], scores, -1e9)
    p = _softmax(scores, axis=-1)
    out = torch.einsum("bhgqk,bhkd->bhgqd", p, v)
    return out.reshape(B, Hq, Sq, D)
```

```cpp
#include <hip/hip_runtime.h>
#include <hip/hip_bf16.h>
#include <stdint.h>

#define KLAYOUT 0
#define KV_AUX 2
#define COMBINE 1

namespace {

typedef unsigned u32x4 __attribute__((ext_vector_type(4)));
typedef unsigned u32x2 __attribute__((ext_vector_type(2)));
typedef float f32x4 __attribute__((ext_vector_type(4)));
typedef float f32x2 __attribute__((ext_vector_type(2)));
typedef __bf16 bf16x8 __attribute__((ext_vector_type(8)));
typedef __bf16 bf16x2 __attribute__((ext_vector_type(2)));
typedef short s16x4 __attribute__((ext_vector_type(4)));

constexpr int NB = 2, HQ = 32, HKV = 4, D = 128, PAGE = 16, NBPS = 2048, SKV = NBPS * PAGE, G = HQ / HKV;
constexpr int NSPLIT = 32, NW = 8, R = 5;
constexpr int NGRP = NB * HKV;
constexpr int PAGE_BYTES = PAGE * HKV * D * 2;
constexpr float SCALE_LOG2E = 0.08838834764831845f * 1.4426950408889634f;
constexpr int WS_EPOCH_STRIDE = 256, WS_SLOTS = 4096, SLOT_BYTES = G * D * 8 + 256, SLOT_ML = G * D * 8;
constexpr int WS_BYTES = WS_SLOTS + NGRP * NSPLIT * SLOT_BYTES;
constexpr int ACC_HS = D + 4, ACC_WS = G * ACC_HS, LDS_M = NW * ACC_WS, LDS_L = LDS_M + G * NW, LDS_FLOATS = LDS_L + G * NW;

__device__ __forceinline__ float xrow_max(float x) {
  auto s = __builtin_amdgcn_permlane16_swap(__float_as_uint(x), __float_as_uint(x), false, false);
  x = fmaxf(__uint_as_float(s[0]), __uint_as_float(s[1]));
  auto t = __builtin_amdgcn_permlane32_swap(__float_as_uint(x), __float_as_uint(x), false, false);
  return fmaxf(__uint_as_float(t[0]), __uint_as_float(t[1]));
}
__device__ __forceinline__ float xrow_sum(float x) {
  auto s = __builtin_amdgcn_permlane16_swap(__float_as_uint(x), __float_as_uint(x), false, false);
  x = __uint_as_float(s[0]) + __uint_as_float(s[1]);
  auto t = __builtin_amdgcn_permlane32_swap(__float_as_uint(x), __float_as_uint(x), false, false);
  return __uint_as_float(t[0]) + __uint_as_float(t[1]);
}
template <int CTRL> __device__ __forceinline__ float dpp(float x) {
  return __builtin_bit_cast(float, __builtin_amdgcn_mov_dpp(__builtin_bit_cast(int, x), CTRL, 0xf, 0xf, true));
}
__device__ __forceinline__ unsigned pack_bf16(float lo, float hi) {
  f32x2 v = {lo, hi};
  return __builtin_bit_cast(unsigned, __builtin_convertvector(v, bf16x2));
}
__device__ __forceinline__ void lds_barrier() {
  asm volatile("s_waitcnt lgkmcnt(0)" ::: "memory");
  __builtin_amdgcn_s_barrier();
  asm volatile("" ::: "memory");
}

struct Ctx {
  const void* kbase; const void* vbase;
  int kv_bytes, k_bytes;
  int koff, voff;
  int npages, len, page0;
};

__device__ __forceinline__ void issue_k(const Ctx& x, int k, int phys, u32x4 (&K)[4]) {
  const bool ok = x.page0 + NSPLIT * NW * k < x.npages;
  const __amdgpu_buffer_rsrc_t rs = __builtin_amdgcn_make_buffer_rsrc((void*)x.kbase, 0, ok ? x.k_bytes : 0, 0x00020000);
  const int so = ok ? phys * PAGE_BYTES : 0;
#pragma unroll
  for (int m = 0; m < 4; ++m) K[m] = __builtin_amdgcn_raw_buffer_load_b128(rs, x.koff + m * (KLAYOUT == 0 ? 64 : 16), so, KV_AUX);
}
__device__ __forceinline__ void issue_v(const Ctx& x, int k, int phys, u32x4 (&V)[4]) {
  const bool ok = x.page0 + NSPLIT * NW * k < x.npages;
  const __amdgpu_buffer_rsrc_t rs = __builtin_amdgcn_make_buffer_rsrc((void*)x.vbase, 0, ok ? x.kv_bytes : 0, 0x00020000);
  const int so = ok ? phys * PAGE_BYTES : 0;
#pragma unroll
  for (int j = 0; j < 4; ++j) V[j] = __builtin_amdgcn_raw_buffer_load_b128(rs, x.voff + j * 1024, so, KV_AUX);
}

template <bool FIRST, bool NEXT>
__device__ __forceinline__ void chunk(const Ctx& x, int k0, const int (&phys)[2 * R], const u32x4 (&Q)[4], u32x4 (&K)[R][4], u32x4 (&V)[R][4],
                                      f32x4 (&acc)[8], float& M, float& l, float* lds, int wave, int lane) {
  const int g = lane >> 4, c = lane & 15;
  f32x4 S[R];
#pragma unroll
  for (int k = 0; k < R; ++k) {
    f32x4 s = {0.f, 0.f, 0.f, 0.f};
#pragma unroll
    for (int m = 0; m < 4; ++m)
      s = __builtin_amdgcn_mfma_f32_16x16x32_bf16(__builtin_bit_cast(bf16x8, K[k][m]), __builtin_bit_cast(bf16x8, Q[m]), s, 0, 0, 0);
    if (NEXT) issue_k(x, k0 + R + k, phys[R + k], K[k]);
    const int rem = x.len - (x.page0 + NSPLIT * NW * (k0 + k)) * PAGE - 4 * g;
#pragma unroll
    for (int i = 0; i < 4; ++i) s[i] = i < rem ? s[i] : -INFINITY;
    S[k] = s;
  }
  float mx = -INFINITY;
#pragma unroll
  for (int k = 0; k < R; ++k) mx = fmaxf(fmaxf(fmaxf(S[k][0], S[k][1]), fmaxf(S[k][2], S[k][3])), mx);
  mx = xrow_max(mx);
  float* lm = lds + LDS_M;
  if (!FIRST) lds_barrier();
  if (lane < G) lm[lane * NW + wave] = mx;
  lds_barrier();
  {
    const f32x4 a = *reinterpret_cast<const f32x4*>(lm + (c & 7) * NW), b = *reinterpret_cast<const f32x4*>(lm + (c & 7) * NW + 4);
    mx = fmaxf(fmaxf(fmaxf(a[0], a[1]), fmaxf(a[2], a[3])), fmaxf(fmaxf(b[0], b[1]), fmaxf(b[2], b[3])));
  }
  const float Mn = FIRST ? mx : fmaxf(M, mx);
  const float msub = (Mn == -INFINITY) ? 0.f : Mn * SCALE_LOG2E;
  if (!FIRST) {
    const float alpha = __builtin_amdgcn_exp2f(M * SCALE_LOG2E - msub);
    l *= alpha;
#pragma unroll
    for (int i = 0; i < 8; ++i) acc[i] *= alpha;
  }
  M = Mn;
  u32x2 P[R];
#pragma unroll
  for (int k = 0; k < R; ++k) {
    float p[4];
#pragma unroll
    for (int i = 0; i < 4; ++i) { p[i] = __builtin_amdgcn_exp2f(fmaf(S[k][i], SCALE_LOG2E, -msub)); l += p[i]; }
    P[k] = u32x2{pack_bf16(p[0], p[1]), pack_bf16(p[2], p[3])};
  }
#pragma unroll
  for (int k = 0; k + 1 < R; k += 2) {
    const u32x4 pb = {P[k][0], P[k][1], P[k + 1][0], P[k + 1][1]};
#pragma unroll
    for (int i = 0; i < 8; ++i) {
      const unsigned sel = (i & 1) ? 0x07060302u : 0x05040100u;
      const u32x4 a = {__builtin_amdgcn_perm(V[k][1][i >> 1], V[k][0][i >> 1], sel), __builtin_amdgcn_perm(V[k][3][i >> 1], V[k][2][i >> 1], sel),
                       __builtin_amdgcn_perm(V[k + 1][1][i >> 1], V[k + 1][0][i >> 1], sel), __builtin_amdgcn_perm(V[k + 1][3][i >> 1], V[k + 1][2][i >> 1], sel)};
      acc[i] = __builtin_amdgcn_mfma_f32_16x16x32_bf16(__builtin_bit_cast(bf16x8, a), __builtin_bit_cast(bf16x8, pb), acc[i], 0, 0, 0);
    }
    if (NEXT) { issue_v(x, k0 + R + k, phys[R + k], V[k]); issue_v(x, k0 + R + k + 1, phys[R + k + 1], V[k + 1]); }
  }
  if (R & 1) {
    constexpr int k = R - 1;
#pragma unroll
    for (int i = 0; i < 8; ++i) {
      const unsigned sel = (i & 1) ? 0x07060302u : 0x05040100u;
      const u32x2 a = {__builtin_amdgcn_perm(V[k][1][i >> 1], V[k][0][i >> 1], sel), __builtin_amdgcn_perm(V[k][3][i >> 1], V[k][2][i >> 1], sel)};
      acc[i] = __builtin_amdgcn_mfma_f32_16x16x16bf16_1k(__builtin_bit_cast(s16x4, a), __builtin_bit_cast(s16x4, P[k]), acc[i], 0, 0, 0);
    }
    if (NEXT) issue_v(x, k0 + R + k, phys[R + k], V[k]);
  }
}

template <bool POLL>
__device__ __forceinline__ void merge_slice(const unsigned char* slots, unsigned tag, int hh, int qd, int lane, __hip_bfloat16* out_row) {
  const int q = lane >> 4, v = lane & 15;
  const __amdgpu_buffer_rsrc_t rs = __builtin_amdgcn_make_buffer_rsrc((void*)slots, 0, NSPLIT * SLOT_BYTES, 0x00020000);
  const int off_ml = (4 * (v & 7) + q) * SLOT_BYTES + SLOT_ML + hh * 16;
  const int off_d = q * SLOT_BYTES + hh * (D * 8) + qd * 256 + v * 16;
  u32x4 ml, d[8];
  for (unsigned spin = 0;; ++spin) {
    asm volatile("" ::: "memory");
    ml = __builtin_amdgcn_raw_buffer_load_b128(rs, off_ml, 0, 16);
#pragma unroll
    for (int it = 0; it < 8; ++it) d[it] = __builtin_amdgcn_raw_buffer_load_b128(rs, off_d, it * 4 * SLOT_BYTES, 16);
    if (!POLL) break;
    bool ok = (ml[1] == tag) & (ml[3] == tag);
#pragma unroll
    for (int it = 0; it < 8; ++it) ok &= (d[it][1] == tag) & (d[it][3] == tag);
    if (__all(ok) || spin > (1u << 20)) break;
    __builtin_amdgcn_s_sleep(1);
  }
  const float m = __uint_as_float(ml[0]);
  float Mx = m;
  Mx = fmaxf(Mx, dpp<0xB1>(Mx)); Mx = fmaxf(Mx, dpp<0x4E>(Mx)); Mx = fmaxf(Mx, dpp<0x141>(Mx));
  Mx = xrow_max(Mx);
  const float w = __builtin_amdgcn_exp2f(m - Mx);
  float L = w * __uint_as_float(ml[2]);
  L += dpp<0xB1>(L); L += dpp<0x4E>(L); L += dpp<0x141>(L);
  L = xrow_sum(L);
  float o0 = 0.f, o1 = 0.f;
#define MRG(IT) { const float wi = dpp<0x150 + IT>(w); o0 = fmaf(wi, __uint_as_float(d[IT][0]), o0); o1 = fmaf(wi, __uint_as_float(d[IT][2]), o1); }
  MRG(0) MRG(1) MRG(2) MRG(3) MRG(4) MRG(5) MRG(6) MRG(7)
#undef MRG
  o0 = xrow_sum(o0); o1 = xrow_sum(o1);
  const float inv = 1.f / L;
  if (lane < 16) *reinterpret_cast<unsigned*>(out_row + 32 * qd + 2 * v) = pack_bf16(o0 * inv, o1 * inv);
}

__global__ void __launch_bounds__(NW * 64, 2)
paged_decode_kernel(const __hip_bfloat16* __restrict__ q, const __hip_bfloat16* __restrict__ kc, const __hip_bfloat16* __restrict__ vc,
                    const int* __restrict__ bt, const int* __restrict__ lens, __hip_bfloat16* __restrict__ out, unsigned char* ws, int kv_bytes) {
  __shared__ __attribute__((aligned(16))) float lds[LDS_FLOATS];
  const int bid = blockIdx.x;
  const int kvh = bid & 3, b = ((bid >> 2) ^ (bid >> 3)) & 1, s = bid >> 3;
  const int grp = b * HKV + kvh;
  const int lane = threadIdx.x & 63, wave = __builtin_amdgcn_readfirstlane(threadIdx.x >> 6), g = lane >> 4, c = lane & 15;

  u32x4 Q[4];
  {
    const __hip_bfloat16* qp = q + (size_t)(b * HQ + kvh * G + (c & 7)) * D + (KLAYOUT == 0 ? 8 * g : 32 * g);
#pragma unroll
    for (int m = 0; m < 4; ++m) Q[m] = *reinterpret_cast<const u32x4*>(qp + m * (KLAYOUT == 0 ? 32 : 8));
  }
  Ctx x;
  x.page0 = s + NSPLIT * wave;
  int phys[2 * R];
#pragma unroll
  for (int k = 0; k < 2 * R; ++k) phys[k] = k < NBPS / (NSPLIT * NW) ? bt[b * NBPS + x.page0 + NSPLIT * NW * k] : 0;
  const int len_raw = lens[b];
  const unsigned tag = *reinterpret_cast<const unsigned*>(ws + grp * WS_EPOCH_STRIDE) + 1u;
  x.len = len_raw <= 0 ? SKV : min(len_raw, SKV);
  x.npages = (x.len + PAGE - 1) / PAGE;
  x.kbase = kc + kvh * D; x.vbase = vc + kvh * D;
  x.kv_bytes = kv_bytes - kvh * D * 2; x.k_bytes = len_raw <= 0 ? 0 : x.kv_bytes;
  x.koff = c * 1024 + g * (KLAYOUT == 0 ? 16 : 64);
  x.voff = g * 4096 + c * 16;

  u32x4 K[R][4], V[R][4];
#pragma unroll
  for (int k = 0; k < R; ++k) issue_k(x, k, phys[k], K[k]);
#pragma unroll
  for (int k = 0; k < R; ++k) issue_v(x, k, phys[k], V[k]);

  f32x4 acc[8];
#pragma unroll
  for (int i = 0; i < 8; ++i) acc[i] = f32x4{0.f, 0.f, 0.f, 0.f};
  float M = -INFINITY, l = 0.f;
  if (x.npages > NSPLIT * NW * R) {
    chunk<true, true>(x, 0, phys, Q, K, V, acc, M, l, lds, wave, lane);
    chunk<false, false>(x, R, phys, Q, K, V, acc, M, l, lds, wave, lane);
  } else {
    chunk<true, false>(x, 0, phys, Q, K, V, acc, M, l, lds, wave, lane);
  }

  l = xrow_sum(l);
  if (lane < G) lds[LDS_L + lane * NW + wave] = l;
  if (c < G) {
    float* a = lds + wave * ACC_WS + c * ACC_HS + 32 * g;
#pragma unroll
    for (int r = 0; r < 4; ++r) {
      *reinterpret_cast<f32x4*>(a + 8 * r) = f32x4{acc[0][r], acc[1][r], acc[2][r], acc[3][r]};
      *reinterpret_cast<f32x4*>(a + 8 * r + 4) = f32x4{acc[4][r], acc[5][r], acc[6][r], acc[7][r]};
    }
  }
  lds_barrier();
  unsigned char* slots = ws + WS_SLOTS + grp * (NSPLIT * SLOT_BYTES);
  {
    const __amdgpu_buffer_rsrc_t rs = __builtin_amdgcn_make_buffer_rsrc((void*)(slots + s * SLOT_BYTES), 0, SLOT_BYTES, 0x00020000);
    float o0 = 0.f, o1 = 0.f;
#pragma unroll
    for (int w = 0; w < NW; ++w) {
      const f32x2 t = *reinterpret_cast<const f32x2*>(lds + w * ACC_WS + wave * ACC_HS + 2 * lane);
      o0 += t[0]; o1 += t[1];
    }
    __builtin_amdgcn_raw_buffer_store_b128(u32x4{__float_as_uint(o0), tag, __float_as_uint(o1), tag}, rs, threadIdx.x * 16, 0, 16);
    if (threadIdx.x < G) {
      const f32x4 a = *reinterpret_cast<const f32x4*>(lds + LDS_L + lane * NW), bb = *reinterpret_cast<const f32x4*>(lds + LDS_L + lane * NW + 4);
      const float L = ((a[0] + a[1]) + (a[2] + a[3])) + ((bb[0] + bb[1]) + (bb[2] + bb[3]));
      __builtin_amdgcn_raw_buffer_store_b128(u32x4{__float_as_uint(M * SCALE_LOG2E), tag, __float_as_uint(L), tag}, rs, SLOT_ML + lane * 16, 0, 16);
    }
  }
  if (wave == NW - 1) {
    merge_slice<true>(slots, tag, s >> 2, s & 3, lane, out + (size_t)(b * HQ + kvh * G + (s >> 2)) * D);
    if (s == 0 && lane == 0) *reinterpret_cast<unsigned*>(ws + grp * WS_EPOCH_STRIDE) = tag;
  }
}

}

extern "C" void kernel_launch(void* const* d_in, const int* in_sizes, int n_in, void* d_out, int out_size, void* d_ws, size_t ws_size,
                              hipStream_t stream) {
  const __hip_bfloat16* q = (const __hip_bfloat16*)d_in[0];
  const __hip_bfloat16* kc = (const __hip_bfloat16*)d_in[1];
  const __hip_bfloat16* vc = (const __hip_bfloat16*)d_in[2];
  const int* bt = (const int*)d_in[3];
  const int* lens = (const int*)d_in[4];
  paged_decode_kernel<<<NGRP * NSPLIT, NW * 64, 0, stream>>>(q, kc, vc, bt, lens, (__hip_bfloat16*)d_out, (unsigned char*)d_ws, in_sizes[1] * 2);
}
```

```cpp
#include <hip/hip_runtime.h>
#include <hip/hip_bf16.h>
#include <stdint.h>

#define KLAYOUT 0
#define KV_AUX 2
#define COMBINE 1

#define STAMP(i)
#define STAMP_ARG
#define STAMP_PASS
#define SB() __builtin_amdgcn_sched_barrier(0)

namespace {

typedef unsigned u32x4 __attribute__((ext_vector_type(4)));
typedef unsigned u32x2 __attribute__((ext_vector_type(2)));
typedef float f32x4 __attribute__((ext_vector_type(4)));
typedef float f32x2 __attribute__((ext_vector_type(2)));
typedef __bf16 bf16x8 __attribute__((ext_vector_type(8)));
typedef __bf16 bf16x2 __attribute__((ext_vector_type(2)));
typedef short s16x4 __attribute__((ext_vector_type(4)));
typedef const __attribute__((address_space(4))) int* cint_p;

constexpr int NB = 2, HQ = 32, HKV = 4, D = 128, PAGE = 16, NBPS = 2048, SKV = NBPS * PAGE, G = HQ / HKV;
constexpr int NSPLIT = 32, NW = 8, R = 5;
constexpr int NGRP = NB * HKV;
constexpr int PAGE_BYTES = PAGE * HKV * D * 2;
constexpr float SCALE_LOG2E = 0.08838834764831845f * 1.4426950408889634f;
constexpr int WS_EPOCH_STRIDE = 256, WS_SLOTS = 4096, SLOT_BYTES = G * D * 8 + 256, SLOT_ML = G * D * 8;
constexpr int WS_BYTES = WS_SLOTS + NGRP * NSPLIT * SLOT_BYTES;
constexpr int ACC_HS = D + 4, ACC_WS = G * ACC_HS, LDS_M = NW * ACC_WS, LDS_L = LDS_M + G * NW, LDS_FLOATS = LDS_L + G * NW;

__device__ __forceinline__ float xrow_max(float x) {
  auto s = __builtin_amdgcn_permlane16_swap(__float_as_uint(x), __float_as_uint(x), false, false);
  x = fmaxf(__uint_as_float(s[0]), __uint_as_float(s[1]));
  auto t = __builtin_amdgcn_permlane32_swap(__float_as_uint(x), __float_as_uint(x), false, false);
  return fmaxf(__uint_as_float(t[0]), __uint_as_float(t[1]));
}
__device__ __forceinline__ float xrow_sum(float x) {
  auto s = __builtin_amdgcn_permlane16_swap(__float_as_uint(x), __float_as_uint(x), false, false);
  x = __uint_as_float(s[0]) + __uint_as_float(s[1]);
  auto t = __builtin_amdgcn_permlane32_swap(__float_as_uint(x), __float_as_uint(x), false, false);
  return __uint_as_float(t[0]) + __uint_as_float(t[1]);
}
template <int CTRL> __device__ __forceinline__ float dpp(float x) {
  return __builtin_bit_cast(float, __builtin_amdgcn_mov_dpp(__builtin_bit_cast(int, x), CTRL, 0xf, 0xf, true));
}
__device__ __forceinline__ unsigned pack_bf16(float lo, float hi) {
  f32x2 v = {lo, hi};
  return __builtin_bit_cast(unsigned, __builtin_convertvector(v, bf16x2));
}
__device__ __forceinline__ void lds_barrier() {
  asm volatile("s_waitcnt lgkmcnt(0)" ::: "memory");
  __builtin_amdgcn_s_barrier();
  asm volatile("" ::: "memory");
}

struct Ctx {
  const void* kbase; const void* vbase;
  int kv_bytes, k_bytes;
  int koff, voff;
  int npages, len, page0;
};

__device__ __forceinline__ void issue_k(const Ctx& x, int k, int phys, u32x4 (&K)[4]) {
  const bool ok = x.page0 + NSPLIT * NW * k < x.npages;
  const __amdgpu_buffer_rsrc_t rs = __builtin_amdgcn_make_buffer_rsrc((void*)x.kbase, 0, ok ? x.k_bytes : 0, 0x00020000);
  const int so = ok ? phys * PAGE_BYTES : 0;
#pragma unroll
  for (int m = 0; m < 4; ++m) K[m] = __builtin_amdgcn_raw_buffer_load_b128(rs, x.koff + m * (KLAYOUT == 0 ? 64 : 16), so, KV_AUX);
}
__device__ __forceinline__ void issue_v(const Ctx& x, int k, int phys, u32x4 (&V)[4]) {
  const bool ok = x.page0 + NSPLIT * NW * k < x.npages;
  const __amdgpu_buffer_rsrc_t rs = __builtin_amdgcn_make_buffer_rsrc((void*)x.vbase, 0, ok ? x.kv_bytes : 0, 0x00020000);
  const int so = ok ? phys * PAGE_BYTES : 0;
#pragma unroll
  for (int j = 0; j < 4; ++j) V[j] = __builtin_amdgcn_raw_buffer_load_b128(rs, x.voff + j * 1024, so, KV_AUX);
}

template <bool FIRST, bool NEXT>
__device__ __forceinline__ void chunk(const Ctx& x, int k0, const int (&phys)[2 * R], const u32x4 (&Q)[4], u32x4 (&K)[R][4], u32x4 (&V)[R][4],
                                      f32x4 (&acc)[8], float& M, float& l, float* lds, int wave, int lane STAMP_ARG) {
  const int g = lane >> 4, c = lane & 15;
  f32x4 S[R];
#pragma unroll
  for (int k = 0; k < R; ++k) {
    f32x4 s = {0.f, 0.f, 0.f, 0.f};
#pragma unroll
    for (int m = 0; m < 4; ++m)
      s = __builtin_amdgcn_mfma_f32_16x16x32_bf16(__builtin_bit_cast(bf16x8, K[k][m]), __builtin_bit_cast(bf16x8, Q[m]), s, 0, 0, 0);
    if (NEXT) issue_k(x, k0 + R + k, phys[R + k], K[k]);
    const int rem = x.len - (x.page0 + NSPLIT * NW * (k0 + k)) * PAGE - 4 * g;
#pragma unroll
    for (int i = 0; i < 4; ++i) s[i] = i < rem ? s[i] : -INFINITY;
    S[k] = s;
  }
  float mx = -INFINITY;
#pragma unroll
  for (int k = 0; k < R; ++k) mx = fmaxf(fmaxf(fmaxf(S[k][0], S[k][1]), fmaxf(S[k][2], S[k][3])), mx);
  mx = xrow_max(mx);
  asm volatile("" :: "v"(mx));
  STAMP(4);
  float* lm = lds + LDS_M;
  if (!FIRST) lds_barrier();
  if (lane < G) lm[lane * NW + wave] = mx;
  lds_barrier();
  STAMP(5);
  {
    const f32x4 a = *reinterpret_cast<const f32x4*>(lm + (c & 7) * NW), b = *reinterpret_cast<const f32x4*>(lm + (c & 7) * NW + 4);
    mx = fmaxf(fmaxf(fmaxf(a[0], a[1]), fmaxf(a[2], a[3])), fmaxf(fmaxf(b[0], b[1]), fmaxf(b[2], b[3])));
  }
  const float Mn = FIRST ? mx : fmaxf(M, mx);
  const float msub = (Mn == -INFINITY) ? 0.f : Mn * SCALE_LOG2E;
  if (!FIRST) {
    const float alpha = __builtin_amdgcn_exp2f(M * SCALE_LOG2E - msub);
    l *= alpha;
#pragma unroll
    for (int i = 0; i < 8; ++i) acc[i] *= alpha;
  }
  M = Mn;
  u32x2 P[R];
#pragma unroll
  for (int k = 0; k < R; ++k) {
    float p[4];
#pragma unroll
    for (int i = 0; i < 4; ++i) { p[i] = __builtin_amdgcn_exp2f(fmaf(S[k][i], SCALE_LOG2E, -msub)); l += p[i]; }
    P[k] = u32x2{pack_bf16(p[0], p[1]), pack_bf16(p[2], p[3])};
  }
#pragma unroll
  for (int k = 0; k + 1 < R; k += 2) {
    const u32x4 pb = {P[k][0], P[k][1], P[k + 1][0], P[k + 1][1]};
#pragma unroll
    for (int i = 0; i < 8; ++i) {
      const unsigned sel = (i & 1) ? 0x07060302u : 0x05040100u;
      const u32x4 a = {__builtin_amdgcn_perm(V[k][1][i >> 1], V[k][0][i >> 1], sel), __builtin_amdgcn_perm(V[k][3][i >> 1], V[k][2][i >> 1], sel),
                       __builtin_amdgcn_perm(V[k + 1][1][i >> 1], V[k + 1][0][i >> 1], sel), __builtin_amdgcn_perm(V[k + 1][3][i >> 1], V[k + 1][2][i >> 1], sel)};
      acc[i] = __builtin_amdgcn_mfma_f32_16x16x32_bf16(__builtin_bit_cast(bf16x8, a), __builtin_bit_cast(bf16x8, pb), acc[i], 0, 0, 0);
    }
    if (NEXT) { issue_v(x, k0 + R + k, phys[R + k], V[k]); issue_v(x, k0 + R + k + 1, phys[R + k + 1], V[k + 1]); }
  }
  if (R & 1) {
    constexpr int k = R - 1;
#pragma unroll
    for (int i = 0; i < 8; ++i) {
      const unsigned sel = (i & 1) ? 0x07060302u : 0x05040100u;
      const u32x2 a = {__builtin_amdgcn_perm(V[k][1][i >> 1], V[k][0][i >> 1], sel), __builtin_amdgcn_perm(V[k][3][i >> 1], V[k][2][i >> 1], sel)};
      acc[i] = __builtin_amdgcn_mfma_f32_16x16x16bf16_1k(__builtin_bit_cast(s16x4, a), __builtin_bit_cast(s16x4, P[k]), acc[i], 0, 0, 0);
    }
    if (NEXT) issue_v(x, k0 + R + k, phys[R + k], V[k]);
  }
}

template <bool POLL>
__device__ __forceinline__ void merge_slice(const unsigned char* slots, unsigned tag, int hh, int qd, int lane, __hip_bfloat16* out_row STAMP_ARG) {
  const int q = lane >> 4, v = lane & 15;
  const __amdgpu_buffer_rsrc_t rs = __builtin_amdgcn_make_buffer_rsrc((void*)slots, 0, NSPLIT * SLOT_BYTES, 0x00020000);
  const int off_ml = (4 * (v & 7) + q) * SLOT_BYTES + SLOT_ML + hh * 16;
  const int off_d = q * SLOT_BYTES + hh * (D * 8) + qd * 256 + v * 16;
  u32x4 ml, d[8];
  for (unsigned spin = 0;; ++spin) {
    asm volatile("" ::: "memory");
    ml = __builtin_amdgcn_raw_buffer_load_b128(rs, off_ml, 0, 16);
#pragma unroll
    for (int it = 0; it < 8; ++it) d[it] = __builtin_amdgcn_raw_buffer_load_b128(rs, off_d, it * 4 * SLOT_BYTES, 16);
    if (!POLL) break;
    bool ok = (ml[1] == tag) & (ml[3] == tag);
#pragma unroll
    for (int it = 0; it < 8; ++it) ok &= (d[it][1] == tag) & (d[it][3] == tag);
    if (__all(ok) || spin > (1u << 20)) break;
    __builtin_amdgcn_s_sleep(1);
  }
  STAMP(9);
  const float m = __uint_as_float(ml[0]);
  float Mx = m;
  Mx = fmaxf(Mx, dpp<0xB1>(Mx)); Mx = fmaxf(Mx, dpp<0x4E>(Mx)); Mx = fmaxf(Mx, dpp<0x141>(Mx));
  Mx = xrow_max(Mx);
  const float w = __builtin_amdgcn_exp2f(m - Mx);
  float L = w * __uint_as_float(ml[2]);
  L += dpp<0xB1>(L); L += dpp<0x4E>(L); L += dpp<0x141>(L);
  L = xrow_sum(L);
  float o0 = 0.f, o1 = 0.f;
#define MRG(IT) { const float wi = dpp<0x150 + IT>(w); o0 = fmaf(wi, __uint_as_float(d[IT][0]), o0); o1 = fmaf(wi, __uint_as_float(d[IT][2]), o1); }
  MRG(0) MRG(1) MRG(2) MRG(3) MRG(4) MRG(5) MRG(6) MRG(7)
#undef MRG
  o0 = xrow_sum(o0); o1 = xrow_sum(o1);
  const float inv = 1.f / L;
  if (lane < 16) *reinterpret_cast<unsigned*>(out_row + 32 * qd + 2 * v) = pack_bf16(o0 * inv, o1 * inv);
}

__global__ void __launch_bounds__(NW * 64, 2)
paged_decode_kernel(const __hip_bfloat16* __restrict__ q, const __hip_bfloat16* __restrict__ kc, const __hip_bfloat16* __restrict__ vc,
                    const int* __restrict__ bt, const int* __restrict__ lens, __hip_bfloat16* __restrict__ out, unsigned char* ws, int kv_bytes) {
  __shared__ __attribute__((aligned(16))) float lds[LDS_FLOATS];
  const unsigned long long tstart = __builtin_amdgcn_s_memrealtime();
  asm volatile("" :: "s"(q), "s"(kc), "s"(vc), "s"(bt), "s"(lens), "s"(out), "s"(ws), "s"(kv_bytes));
  STAMP(1);
  const int bid = blockIdx.x;
  const int kvh = bid & 3, b = ((bid >> 2) ^ (bid >> 3)) & 1, s = bid >> 3;
  const int grp = b * HKV + kvh;
  const int lane = threadIdx.x & 63, wave = __builtin_amdgcn_readfirstlane(threadIdx.x >> 6), g = lane >> 4, c = lane & 15;

  u32x4 Q[4];
  {
    const __hip_bfloat16* qp = q + (size_t)(b * HQ + kvh * G + (c & 7)) * D + (KLAYOUT == 0 ? 8 * g : 32 * g);
#pragma unroll
    for (int m = 0; m < 4; ++m) Q[m] = *reinterpret_cast<const u32x4*>(qp + m * (KLAYOUT == 0 ? 32 : 8));
  }
  SB();
  Ctx x;
  x.page0 = s + NSPLIT * wave;
  int phys[2 * R];
#pragma unroll
  for (int k = 0; k < 2 * R; ++k) phys[k] = k < NBPS / (NSPLIT * NW) ? ((cint_p)bt)[b * NBPS + x.page0 + NSPLIT * NW * k] : 0;
  const int len_raw = ((cint_p)lens)[b];
  unsigned tag = (unsigned)*(cint_p)(ws + grp * WS_EPOCH_STRIDE) + 1u;
  asm volatile("" : "+s"(tag));
  x.len = len_raw <= 0 ? SKV : min(len_raw, SKV);
  x.npages = (x.len + PAGE - 1) / PAGE;
  x.kbase = kc + kvh * D; x.vbase = vc + kvh * D;
  x.kv_bytes = kv_bytes - kvh * D * 2; x.k_bytes = len_raw <= 0 ? 0 : x.kv_bytes;
  x.koff = c * 1024 + g * (KLAYOUT == 0 ? 16 : 64);
  x.voff = g * 4096 + c * 16;

  asm volatile("" :: "s"(phys[0]), "s"(len_raw), "s"(tag));
  STAMP(2);
  u32x4 K[R][4], V[R][4];
#pragma unroll
  for (int k = 0; k < R; ++k) { issue_k(x, k, phys[k], K[k]); SB(); }
#pragma unroll
  for (int k = 0; k < R; ++k) { issue_v(x, k, phys[k], V[k]); SB(); }
  STAMP(3);

  f32x4 acc[8];
#pragma unroll
  for (int i = 0; i < 8; ++i) acc[i] = f32x4{0.f, 0.f, 0.f, 0.f};
  float M = -INFINITY, l = 0.f;
  if (x.npages > NSPLIT * NW * R) {
    chunk<true, true>(x, 0, phys, Q, K, V, acc, M, l, lds, wave, lane STAMP_PASS);
    chunk<false, false>(x, R, phys, Q, K, V, acc, M, l, lds, wave, lane STAMP_PASS);
  } else {
    chunk<true, false>(x, 0, phys, Q, K, V, acc, M, l, lds, wave, lane STAMP_PASS);
  }

  asm volatile("" :: "v"(acc[7]));
  STAMP(6);
  l = xrow_sum(l);
  if (lane < G) lds[LDS_L + lane * NW + wave] = l;
  if (c < G) {
    float* a = lds + wave * ACC_WS + c * ACC_HS + 32 * g;
#pragma unroll
    for (int r = 0; r < 4; ++r) {
      *reinterpret_cast<f32x4*>(a + 8 * r) = f32x4{acc[0][r], acc[1][r], acc[2][r], acc[3][r]};
      *reinterpret_cast<f32x4*>(a + 8 * r + 4) = f32x4{acc[4][r], acc[5][r], acc[6][r], acc[7][r]};
    }
  }
  lds_barrier();
  STAMP(7);
  unsigned char* slots = ws + WS_SLOTS + grp * (NSPLIT * SLOT_BYTES);
  {
    const __amdgpu_buffer_rsrc_t rs = __builtin_amdgcn_make_buffer_rsrc((void*)(slots + s * SLOT_BYTES), 0, SLOT_BYTES, 0x00020000);
    float o0 = 0.f, o1 = 0.f;
#pragma unroll
    for (int w = 0; w < NW; ++w) {
      const f32x2 t = *reinterpret_cast<const f32x2*>(lds + w * ACC_WS + wave * ACC_HS + 2 * lane);
      o0 += t[0]; o1 += t[1];
    }
    __builtin_amdgcn_raw_buffer_store_b128(u32x4{__float_as_uint(o0), tag, __float_as_uint(o1), tag}, rs, threadIdx.x * 16, 0, 16);
    if (threadIdx.x < G) {
      const f32x4 a = *reinterpret_cast<const f32x4*>(lds + LDS_L + lane * NW), bb = *reinterpret_cast<const f32x4*>(lds + LDS_L + lane * NW + 4);
      const float L = ((a[0] + a[1]) + (a[2] + a[3])) + ((bb[0] + bb[1]) + (bb[2] + bb[3]));
      __builtin_amdgcn_raw_buffer_store_b128(u32x4{__float_as_uint(M * SCALE_LOG2E), tag, __float_as_uint(L), tag}, rs, SLOT_ML + lane * 16, 0, 16);
    }
  }
  STAMP(8);
  if (wave == NW - 1) {
    merge_slice<true>(slots, tag, s >> 2, s & 3, lane, out + (size_t)(b * HQ + kvh * G + (s >> 2)) * D STAMP_PASS);
    while (__builtin_amdgcn_s_memrealtime() < tstart + 4000) __builtin_amdgcn_s_sleep(1);
    if (s == 0 && lane == 0) *reinterpret_cast<unsigned*>(ws + grp * WS_EPOCH_STRIDE) = tag;
  }
}

}

extern "C" void kernel_launch(void* const* d_in, const int* in_sizes, int n_in, void* d_out, int out_size, void* d_ws, size_t ws_size,
                              hipStream_t stream) {
  const __hip_bfloat16* q = (const __hip_bfloat16*)d_in[0];
  const __hip_bfloat16* kc = (const __hip_bfloat16*)d_in[1];
  const __hip_bfloat16* vc = (const __hip_bfloat16*)d_in[2];
  const int* bt = (const int*)d_in[3];
  const int* lens = (const int*)d_in[4];
  paged_decode_kernel<<<NGRP * NSPLIT, NW * 64, 0, stream>>>(q, kc, vc, bt, lens, (__hip_bfloat16*)d_out, (unsigned char*)d_ws, in_sizes[1] * 2);
}
```

```cpp
#include <hip/hip_runtime.h>
#include <hip/hip_bf16.h>
#include <stdint.h>

namespace {

constexpr int NT = 9 * 64, NWG = 256;
constexpr int LDS_FLOATS = 21 * 1024;

__global__ void __launch_bounds__(NT, 3)
paged_decode_kernel(const void* q, const void* kc, const void* vc, const int* bt, const int* lens, void* out, void* ws, int kv_bytes, int unused) {
  __shared__ __attribute__((aligned(16))) float lds[LDS_FLOATS];
  asm volatile(
      "s_load_dwordx16 s[4:19], %0, 0x0\n"
      "s_memrealtime s[88:89]\n"
      "s_getpc_b64 s[60:61]\n"
      ".Lpd7_%=:\n"
      "v_lshlrev_b32 v3, 7, %2\n"
      "v_cmp_gt_u32 vcc, .Lpd6_%=-.Lpd7_%=, v3\n"
      "s_and_saveexec_b64 s[62:63], vcc\n"
      "s_cbranch_execz .Lpd8_%=\n"
      "global_load_dword v3, v3, s[60:61]\n"
      ".Lpd8_%=:\n"
      "s_mov_b64 exec, -1\n"
      "v_readfirstlane_b32 s36, %2\n"
      "v_lshlrev_b32 v1, 2, %2\n"
      "v_mov_b32 v93, 0\n"
      "ds_write_b32 v1, v93 offset:40960\n"
      "v_and_b32 v1, 63, %2\n"
      "s_bfe_u32 s37, %1, 0x30003\n"
      "s_lshr_b32 s52, %1, 3\n"
      "s_and_b32 s52, s52, 24\n"
      "s_and_b32 s38, %1, 7\n"
      "s_or_b32 s38, s38, s52\n"
      "s_lshr_b32 s36, s36, 6\n"
      "s_cmp_lg_u32 s36, 8\n"
      "s_cbranch_scc1 .Lpd9_%=\n"
      "s_waitcnt lgkmcnt(0)\n"
      "s_barrier\n"
      "s_barrier\n"
      "s_memrealtime s[88:89]\n"
      "s_branch .Lpd1_%=\n"
      ".Lpd9_%=:\n"
      "s_lshl_b32 s39, s36, 5\n"
      "s_add_u32 s39, s39, s38\n"
      "s_and_b32 s52, s37, 4\n"
      "s_lshl_b32 s52, s52, 9\n"
      "s_add_u32 s52, s52, s39\n"
      "s_lshl_b32 s52, s52, 2\n"
      "s_and_b32 s53, s52, 0x2000\n"
      "v_lshl_add_u32 v95, %2, 4, s53\n"
      "v_and_b32 v94, 7, v1\n"
      "v_lshlrev_b32 v96, 1, v1\n"
      "v_and_b32 v96, 0x70, v96\n"
      "v_lshl_or_b32 v96, v94, 8, v96\n"
      "s_lshl_b32 s53, s37, 11\n"
      "v_or_b32 v96, s53, v96\n"
      "s_and_b32 s54, s37, 3\n"
      "s_lshl_b32 s54, s54, 8\n"
      "s_mov_b32 s23, 0x20000\n"
      "s_waitcnt lgkmcnt(0)\n"
      "s_load_dword s41, s[10:11], s52\n"
      "s_load_dwordx2 s[50:51], s[12:13], 0x0\n"
      "global_load_dwordx4 v[76:79], v95, s[10:11]\n"
      "s_barrier\n"
      "global_load_dwordx4 v[4:7], v96, s[4:5]\n"
      "global_load_dwordx4 v[8:11], v96, s[4:5] offset:128\n"
      "s_add_u32 s20, s6, s54\n"
      "s_addc_u32 s21, s7, 0\n"
      "s_and_b32 s21, s21, 0xffff\n"
      "s_sub_u32 s22, s18, s54\n"
      "v_and_b32 v97, 14, v1\n"
      "v_lshlrev_b32 v116, 9, v97\n"
      "v_and_b32 v97, 48, v1\n"
      "v_lshl_or_b32 v116, v97, 1, v116\n"
      "v_and_b32 v98, 1, v1\n"
      "v_lshl_or_b32 v116, v98, 4, v116\n"
      "v_or_b32 v117, 0x2000, v116\n"
      "s_add_u32 s24, s8, s54\n"
      "s_addc_u32 s25, s9, 0\n"
      "s_and_b32 s25, s25, 0xffff\n"
      "s_mov_b32 s26, s22\n"
      "s_mov_b32 s27, 0x20000\n"
      "v_and_b32 v98, 15, v1\n"
      "v_lshlrev_b32 v118, 4, v98\n"
      "v_lshl_or_b32 v118, v97, 7, v118\n"
      "v_or_b32 v119, 0x2000, v118\n"
      "s_mov_b32 s47, 0x5040100\n"
      "s_mov_b32 s48, 0x7060302\n"
      "v_mov_b32 v88, 0xf149f2ca\n"
      "v_mov_b32 v89, 0\n"
      "s_and_b32 s53, s20, 0x300\n"
      "s_cmp_eq_u32 s53, 0x100\n"
      "s_movk_i32 s44, 0\n"
      "s_cselect_b32 s44, s44, 10240\n"
      "v_lshrrev_b32 v120, 3, v97\n"
      "v_lshlrev_b32 v94, 2, %2\n"
      "v_lshlrev_b32 v99, 4, v1\n"
      "v_mov_b64 v[44:45], 0\n"
      "v_mov_b64 v[46:47], 0\n"
      "v_mov_b64 v[48:49], 0\n"
      "v_mov_b64 v[50:51], 0\n"
      "v_mov_b64 v[52:53], 0\n"
      "v_mov_b64 v[54:55], 0\n"
      "v_mov_b64 v[56:57], 0\n"
      "v_mov_b64 v[58:59], 0\n"
      "v_mov_b64 v[60:61], 0\n"
      "v_mov_b64 v[62:63], 0\n"
      "v_mov_b64 v[64:65], 0\n"
      "v_mov_b64 v[66:67], 0\n"
      "v_mov_b64 v[68:69], 0\n"
      "v_mov_b64 v[70:71], 0\n"
      "v_mov_b64 v[72:73], 0\n"
      "v_mov_b64 v[74:75], 0\n"
      "s_waitcnt lgkmcnt(0)\n"
      "s_lshl_b32 s40, s41, 14\n"
      "buffer_load_dwordx4 v[12:15], v116, s[20:23], s40 offen nt\n"
      "buffer_load_dwordx4 v[16:19], v116, s[20:23], s40 offen offset:128 nt\n"
      "buffer_load_dwordx4 v[20:23], v117, s[20:23], s40 offen nt\n"
      "buffer_load_dwordx4 v[24:27], v117, s[20:23], s40 offen offset:128 nt\n"
      "s_mov_b32 s12, s52\n"
      "s_cmp_ge_u32 s37, 4\n"
      "s_cselect_b32 s52, s51, s50\n"
      "s_cmp_gt_i32 s52, 0\n"
      "s_cselect_b32 s46, 0x3e0293ee, 0\n"
      "s_cselect_b32 s52, s52, 0x8000\n"
      "s_min_i32 s52, s52, 0x8000\n"
      "s_sub_u32 s9, s52, 1\n"
      "s_lshr_b32 s9, s9, 4\n"
      "v_sub_u32 v94, s9, v94\n"
      "s_cmp_eq_u32 s36, 0\n"
      "s_cbranch_scc1 .Lpd10_%=\n"
      "s_waitcnt vmcnt(6)\n"
      "v_lshrrev_b32 v100, 3, v76\n"
      "v_lshlrev_b32_e64 v101, v76, 1\n"
      "v_and_b32 v100, 0x3fc, v100\n"
      "v_cmp_lt_i32 vcc, 0, v94\n"
      "s_mov_b64 exec, vcc\n"
      "ds_or_b32 v100, v101 offset:40960\n"
      "v_lshrrev_b32 v102, 3, v77\n"
      "v_lshlrev_b32_e64 v103, v77, 1\n"
      "v_and_b32 v102, 0x3fc, v102\n"
      "v_cmp_lt_i32 vcc, 1, v94\n"
      "s_mov_b64 exec, vcc\n"
      "ds_or_b32 v102, v103 offset:40960\n"
      "v_lshrrev_b32 v104, 3, v78\n"
      "v_lshlrev_b32_e64 v105, v78, 1\n"
      "v_and_b32 v104, 0x3fc, v104\n"
      "v_cmp_lt_i32 vcc, 2, v94\n"
      "s_mov_b64 exec, vcc\n"
      "ds_or_b32 v104, v105 offset:40960\n"
      "v_lshrrev_b32 v106, 3, v79\n"
      "v_lshlrev_b32_e64 v107, v79, 1\n"
      "v_and_b32 v106, 0x3fc, v106\n"
      "v_cmp_lt_i32 vcc, 3, v94\n"
      "s_mov_b64 exec, vcc\n"
      "ds_or_b32 v106, v107 offset:40960\n"
      "s_mov_b64 exec, -1\n"
      ".Lpd10_%=:\n"
      "s_sub_u32 s53, s9, s39\n"
      "s_add_u32 s53, s53, 256\n"
      "s_lshr_b32 s43, s53, 8\n"
      "s_cmp_gt_i32 s50, 0\n"
      "s_cselect_b32 s50, s50, 0x8000\n"
      "s_min_i32 s50, s50, 0x8000\n"
      "s_cmp_gt_i32 s51, 0\n"
      "s_cselect_b32 s51, s51, 0x8000\n"
      "s_min_i32 s51, s51, 0x8000\n"
      "s_add_u32 s54, s50, s51\n"
      "v_cvt_f32_u32 v94, s54\n"
      "s_lshl_b32 s53, s43, 3\n"
      "s_sub_u32 s53, s53, 4\n"
      "v_cvt_f32_u32 v95, s53\n"
      "v_mul_f32 v94, 0x40cfff05, v94\n"
      "v_rcp_f32 v95, v95\n"
      "s_lshl_b32 s53, s43, 8\n"
      "s_add_u32 s53, s53, s39\n"
      "s_lshl_b32 s53, s53, 4\n"
      "s_sub_u32 s52, s52, s53\n"
      "s_add_u32 s52, s52, 4096\n"
      "v_mul_f32 v94, v94, v95\n"
      "v_cvt_u32_f32 v94, v94\n"
      "v_sub_u32 v120, s52, v120\n"
      "s_sub_i32 s9, s9, 256\n"
      "s_mov_b32 s8, s39\n"
      "v_readfirstlane_b32 s93, v94\n"
      "s_cmp_eq_u32 s43, 0\n"
      "s_waitcnt lgkmcnt(0)\n"
      "s_barrier\n"
      "ds_read_b128 v[128:131], v99 offset:40960\n"
      "s_memrealtime s[88:89]\n"
      "s_cbranch_scc1 .Lpd2_%=\n"
      "s_waitcnt lgkmcnt(0)\n"
      "s_add_u32 s92, s88, 0\n"
      "s_lshl_b32 s92, s92, 8\n"
      "s_mov_b32 s42, 1\n"
      "s_waitcnt lgkmcnt(0)\n"
      "v_bcnt_u32_b32 v133, v128, 0\n"
      "v_bcnt_u32_b32 v133, v129, v133\n"
      "v_bcnt_u32_b32 v133, v130, v133\n"
      "v_bcnt_u32_b32 v133, v131, v133\n"
      "s_nop 1\n"
      "v_add_u32_dpp v132, v133, v133 row_shr:1 row_mask:0xf bank_mask:0xf bound_ctrl:1\n"
      "s_nop 1\n"
      "v_add_u32_dpp v132, v132, v132 row_shr:2 row_mask:0xf bank_mask:0xf bound_ctrl:1\n"
      "s_nop 1\n"
      "v_add_u32_dpp v132, v132, v132 row_shr:4 row_mask:0xf bank_mask:0xf bound_ctrl:1\n"
      "s_nop 1\n"
      "v_add_u32_dpp v132, v132, v132 row_shr:8 row_mask:0xf bank_mask:0xf bound_ctrl:1\n"
      "s_nop 1\n"
      "v_add_u32_dpp v132, v132, v132 row_bcast:15 row_mask:0xa bank_mask:0xf\n"
      "s_nop 1\n"
      "v_add_u32_dpp v132, v132, v132 row_bcast:31 row_mask:0xc bank_mask:0xf\n"
      "v_sub_u32 v133, v132, v133\n"
      "v_readlane_b32 s52, v132, 63\n"
      "s_cmp_gt_u32 s18, 134217728\n"
      "s_cselect_b32 s52, -1, s52\n"
      "s_cmp_eq_u32 s52, s9\n"
      "s_cselect_b32 s9, s9, 0\n"
      "s_cmp_lt_u32 s42, s43\n"
      "s_cbranch_scc0 .Lpd5_%=\n"
      ".Lpd4_%=:\n"
      "s_memrealtime s[90:91]\n"
      "s_add_u32 s92, s92, s93\n"
      "s_waitcnt lgkmcnt(0)\n"
      "s_lshl_b32 s90, s90, 8\n"
      "s_sub_i32 s90, s90, s92\n"
      "s_cmp_ge_i32 s90, s44\n"
      "s_waitcnt vmcnt(3)\n"
      "s_cbranch_scc1 .Lpd13_%=\n"
      "s_cmp_lt_i32 s90, 0\n"
      "s_waitcnt vmcnt(2)\n"
      "s_cbranch_scc0 .Lpd13_%=\n"
      ".Lpd12_%=:\n"
      "s_memrealtime s[90:91]\n"
      "s_waitcnt lgkmcnt(0)\n"
      "s_lshl_b32 s90, s90, 8\n"
      "s_sub_i32 s90, s90, s92\n"
      "s_cmp_lt_i32 s90, 0\n"
      "s_cbranch_scc1 .Lpd12_%=\n"
      ".Lpd13_%=:\n"
      "buffer_load_dwordx4 v[28:31], v118, s[24:27], s40 offen nt\n"
      "s_memrealtime s[90:91]\n"
      "s_add_u32 s92, s92, s93\n"
      "s_waitcnt lgkmcnt(0)\n"
      "s_lshl_b32 s90, s90, 8\n"
      "s_sub_i32 s90, s90, s92\n"
      "s_cmp_ge_i32 s90, s44\n"
      "s_waitcnt vmcnt(3)\n"
      "s_cbranch_scc1 .Lpd16_%=\n"
      "s_cmp_lt_i32 s90, 0\n"
      "s_waitcnt vmcnt(2)\n"
      "s_cbranch_scc0 .Lpd16_%=\n"
      ".Lpd15_%=:\n"
      "s_memrealtime s[90:91]\n"
      "s_waitcnt lgkmcnt(0)\n"
      "s_lshl_b32 s90, s90, 8\n"
      "s_sub_i32 s90, s90, s92\n"
      "s_cmp_lt_i32 s90, 0\n"
      "s_cbranch_scc1 .Lpd15_%=\n"
      ".Lpd16_%=:\n"
      "buffer_load_dwordx4 v[32:35], v118, s[24:27], s40 offen offset:1024 nt\n"
      "v_mfma_f32_16x16x32_bf16 v[76:79], v[12:15], v[4:7], 0\n"
      "v_mfma_f32_16x16x32_bf16 v[76:79], v[16:19], v[8:11], v[76:79]\n"
      "v_cmp_lt_u32 vcc, s8, v132\n"
      "s_add_u32 s12, s12, 1024\n"
      "s_cmp_lt_i32 s8, s9\n"
      "s_cbranch_scc1 .Lpd17_%=\n"
      "s_load_dword s13, s[10:11], s12\n"
      ".Lpd17_%=:\n"
      "s_ff1_i32_b64 s52, vcc\n"
      "v_readlane_b32 s4, v128, s52\n"
      "v_readlane_b32 s5, v129, s52\n"
      "v_readlane_b32 s6, v130, s52\n"
      "v_readlane_b32 s7, v131, s52\n"
      "v_readlane_b32 s53, v133, s52\n"
      "s_lshl_b32 s52, s52, 7\n"
      "s_bcnt1_i32_b64 s54, s[4:5]\n"
      "s_sub_u32 s53, s8, s53\n"
      "s_cmp_ge_u32 s53, s54\n"
      "s_cselect_b64 s[60:61], s[6:7], s[4:5]\n"
      "s_cselect_b32 s54, s54, 0\n"
      "s_cselect_b32 s55, 64, 0\n"
      "v_mbcnt_lo_u32_b32 v94, s60, 0\n"
      "s_sub_u32 s53, s53, s54\n"
      "v_mbcnt_hi_u32_b32 v94, s61, v94\n"
      "s_add_u32 s52, s52, s55\n"
      "v_cmp_eq_u32 vcc, s53, v94\n"
      "s_and_b64 s[60:61], s[60:61], vcc\n"
      "s_ff1_i32_b64 s53, s[60:61]\n"
      "s_add_u32 s52, s52, s53\n"
      "s_memrealtime s[90:91]\n"
      "s_add_u32 s92, s92, s93\n"
      "s_waitcnt lgkmcnt(0)\n"
      "s_lshl_b32 s90, s90, 8\n"
      "s_sub_i32 s90, s90, s92\n"
      "s_cmp_ge_i32 s90, s44\n"
      "s_waitcnt vmcnt(3)\n"
      "s_cbranch_scc1 .Lpd20_%=\n"
      "s_cmp_lt_i32 s90, 0\n"
      "s_waitcnt vmcnt(2)\n"
      "s_cbranch_scc0 .Lpd20_%=\n"
      ".Lpd19_%=:\n"
      "s_memrealtime s[90:91]\n"
      "s_waitcnt lgkmcnt(0)\n"
      "s_lshl_b32 s90, s90, 8\n"
      "s_sub_i32 s90, s90, s92\n"
      "s_cmp_lt_i32 s90, 0\n"
      "s_cbranch_scc1 .Lpd19_%=\n"
      ".Lpd20_%=:\n"
      "buffer_load_dwordx4 v[36:39], v119, s[24:27], s40 offen nt\n"
      "s_cmp_lt_i32 s8, s9\n"
      "s_cselect_b32 s52, s52, s13\n"
      "s_add_u32 s8, s8, 256\n"
      "s_lshl_b32 s41, s52, 14\n"
      "s_add_u32 s42, s42, 1\n"
      "s_memrealtime s[90:91]\n"
      "s_add_u32 s92, s92, s93\n"
      "s_waitcnt lgkmcnt(0)\n"
      "s_lshl_b32 s90, s90, 8\n"
      "s_sub_i32 s90, s90, s92\n"
      "s_cmp_ge_i32 s90, s44\n"
      "s_waitcnt vmcnt(3)\n"
      "s_cbranch_scc1 .Lpd23_%=\n"
      "s_cmp_lt_i32 s90, 0\n"
      "s_waitcnt vmcnt(2)\n"
      "s_cbranch_scc0 .Lpd23_%=\n"
      ".Lpd22_%=:\n"
      "s_memrealtime s[90:91]\n"
      "s_waitcnt lgkmcnt(0)\n"
      "s_lshl_b32 s90, s90, 8\n"
      "s_sub_i32 s90, s90, s92\n"
      "s_cmp_lt_i32 s90, 0\n"
      "s_cbranch_scc1 .Lpd22_%=\n"
      ".Lpd23_%=:\n"
      "buffer_load_dwordx4 v[40:43], v119, s[24:27], s40 offen offset:1024 nt\n"
      "v_mfma_f32_16x16x32_bf16 v[80:83], v[20:23], v[4:7], 0\n"
      "v_add_f32_dpp v84, v77, v76 row_ror:8 row_mask:0xf bank_mask:0xf\n"
      "v_mfma_f32_16x16x32_bf16 v[80:83], v[24:27], v[8:11], v[80:83]\n"
      "v_add_f32_dpp v85, v79, v78 row_ror:8 row_mask:0xf bank_mask:0xf\n"
      "v_max_f32 v94, v84, v85\n"
      "s_nop 5\n"
      "v_add_f32_dpp v86, v81, v80 row_ror:8 row_mask:0xf bank_mask:0xf\n"
      "v_add_f32_dpp v87, v83, v82 row_ror:8 row_mask:0xf bank_mask:0xf\n"
      "v_max3_f32 v94, v94, v86, v87\n"
      "v_mov_b32 v95, v94\n"
      "s_nop 1\n"
      "v_permlane16_swap_b32 v94, v95\n"
      "v_max_f32 v94, v94, v95\n"
      "v_mov_b32 v95, v94\n"
      "s_nop 1\n"
      "v_permlane32_swap_b32 v94, v95\n"
      "v_max_f32 v94, v94, v95\n"
      "v_mul_f32 v94, s46, v94\n"
      "v_max_f32 v96, v88, v94\n"
      "v_sub_f32 v92, v88, v96\n"
      "v_fma_f32 v84, v84, s46, -v96\n"
      "v_fma_f32 v85, v85, s46, -v96\n"
      "v_fma_f32 v86, v86, s46, -v96\n"
      "v_fma_f32 v87, v87, s46, -v96\n"
      "v_exp_f32 v92, v92\n"
      "v_exp_f32 v84, v84\n"
      "v_exp_f32 v85, v85\n"
      "v_exp_f32 v86, v86\n"
      "v_exp_f32 v87, v87\n"
      "v_mov_b32 v88, v96\n"
      "v_add_f32 v94, v84, v85\n"
      "v_add_f32 v95, v86, v87\n"
      "v_cvt_pk_bf16_f32 v90, v84, v85\n"
      "v_add_f32 v94, v94, v95\n"
      "v_cvt_pk_bf16_f32 v91, v86, v87\n"
      "v_fma_f32 v89, v89, v92, v94\n"
      "s_memrealtime s[90:91]\n"
      "s_add_u32 s92, s92, s93\n"
      "s_waitcnt lgkmcnt(0)\n"
      "s_lshl_b32 s90, s90, 8\n"
      "s_sub_i32 s90, s90, s92\n"
      "s_cmp_ge_i32 s90, s44\n"
      "s_waitcnt vmcnt(3)\n"
      "s_cbranch_scc1 .Lpd26_%=\n"
      "s_cmp_lt_i32 s90, 0\n"
      "s_waitcnt vmcnt(2)\n"
      "s_cbranch_scc0 .Lpd26_%=\n"
      ".Lpd25_%=:\n"
      "s_memrealtime s[90:91]\n"
      "s_waitcnt lgkmcnt(0)\n"
      "s_lshl_b32 s90, s90, 8\n"
      "s_sub_i32 s90, s90, s92\n"
      "s_cmp_lt_i32 s90, 0\n"
      "s_cbranch_scc1 .Lpd25_%=\n"
      ".Lpd26_%=:\n"
      "buffer_load_dwordx4 v[12:15], v116, s[20:23], s41 offen nt\n"
      "v_pk_mul_f32 v[44:45], v[44:45], v[92:93] op_sel_hi:[1,0]\n"
      "v_pk_mul_f32 v[46:47], v[46:47], v[92:93] op_sel_hi:[1,0]\n"
      "v_pk_mul_f32 v[48:49], v[48:49], v[92:93] op_sel_hi:[1,0]\n"
      "v_pk_mul_f32 v[50:51], v[50:51], v[92:93] op_sel_hi:[1,0]\n"
      "v_pk_mul_f32 v[52:53], v[52:53], v[92:93] op_sel_hi:[1,0]\n"
      "v_pk_mul_f32 v[54:55], v[54:55], v[92:93] op_sel_hi:[1,0]\n"
      "v_pk_mul_f32 v[56:57], v[56:57], v[92:93] op_sel_hi:[1,0]\n"
      "v_pk_mul_f32 v[58:59], v[58:59], v[92:93] op_sel_hi:[1,0]\n"
      "v_pk_mul_f32 v[60:61], v[60:61], v[92:93] op_sel_hi:[1,0]\n"
      "v_pk_mul_f32 v[62:63], v[62:63], v[92:93] op_sel_hi:[1,0]\n"
      "v_pk_mul_f32 v[64:65], v[64:65], v[92:93] op_sel_hi:[1,0]\n"
      "v_pk_mul_f32 v[66:67], v[66:67], v[92:93] op_sel_hi:[1,0]\n"
      "v_pk_mul_f32 v[68:69], v[68:69], v[92:93] op_sel_hi:[1,0]\n"
      "v_pk_mul_f32 v[70:71], v[70:71], v[92:93] op_sel_hi:[1,0]\n"
      "v_pk_mul_f32 v[72:73], v[72:73], v[92:93] op_sel_hi:[1,0]\n"
      "v_pk_mul_f32 v[74:75], v[74:75], v[92:93] op_sel_hi:[1,0]\n"
      "s_memrealtime s[90:91]\n"
      "s_add_u32 s92, s92, s93\n"
      "s_waitcnt lgkmcnt(0)\n"
      "s_lshl_b32 s90, s90, 8\n"
      "s_sub_i32 s90, s90, s92\n"
      "s_cmp_ge_i32 s90, s44\n"
      "s_waitcnt vmcnt(3)\n"
      "s_cbranch_scc1 .Lpd29_%=\n"
      "s_cmp_lt_i32 s90, 0\n"
      "s_waitcnt vmcnt(2)\n"
      "s_cbranch_scc0 .Lpd29_%=\n"
      ".Lpd28_%=:\n"
      "s_memrealtime s[90:91]\n"
      "s_waitcnt lgkmcnt(0)\n"
      "s_lshl_b32 s90, s90, 8\n"
      "s_sub_i32 s90, s90, s92\n"
      "s_cmp_lt_i32 s90, 0\n"
      "s_cbranch_scc1 .Lpd28_%=\n"
      ".Lpd29_%=:\n"
      "buffer_load_dwordx4 v[16:19], v116, s[20:23], s41 offen offset:128 nt\n"
      "v_perm_b32 v100, v32, v28, s47\n"
      "v_perm_b32 v102, v32, v28, s48\n"
      "v_perm_b32 v104, v33, v29, s47\n"
      "v_perm_b32 v106, v33, v29, s48\n"
      "v_perm_b32 v108, v34, v30, s47\n"
      "v_perm_b32 v110, v34, v30, s48\n"
      "v_perm_b32 v112, v35, v31, s47\n"
      "v_perm_b32 v114, v35, v31, s48\n"
      "s_memrealtime s[90:91]\n"
      "s_add_u32 s92, s92, s93\n"
      "s_waitcnt lgkmcnt(0)\n"
      "s_lshl_b32 s90, s90, 8\n"
      "s_sub_i32 s90, s90, s92\n"
      "s_cmp_ge_i32 s90, s44\n"
      "s_waitcnt vmcnt(3)\n"
      "s_cbranch_scc1 .Lpd32_%=\n"
      "s_cmp_lt_i32 s90, 0\n"
      "s_waitcnt vmcnt(2)\n"
      "s_cbranch_scc0 .Lpd32_%=\n"
      ".Lpd31_%=:\n"
      "s_memrealtime s[90:91]\n"
      "s_waitcnt lgkmcnt(0)\n"
      "s_lshl_b32 s90, s90, 8\n"
      "s_sub_i32 s90, s90, s92\n"
      "s_cmp_lt_i32 s90, 0\n"
      "s_cbranch_scc1 .Lpd31_%=\n"
      ".Lpd32_%=:\n"
      "buffer_load_dwordx4 v[20:23], v117, s[20:23], s41 offen nt\n"
      "s_memrealtime s[90:91]\n"
      "s_add_u32 s92, s92, s93\n"
      "s_waitcnt lgkmcnt(0)\n"
      "s_lshl_b32 s90, s90, 8\n"
      "s_sub_i32 s90, s90, s92\n"
      "s_cmp_ge_i32 s90, s44\n"
      "s_waitcnt vmcnt(3)\n"
      "s_cbranch_scc1 .Lpd35_%=\n"
      "s_cmp_lt_i32 s90, 0\n"
      "s_waitcnt vmcnt(2)\n"
      "s_cbranch_scc0 .Lpd35_%=\n"
      ".Lpd34_%=:\n"
      "s_memrealtime s[90:91]\n"
      "s_waitcnt lgkmcnt(0)\n"
      "s_lshl_b32 s90, s90, 8\n"
      "s_sub_i32 s90, s90, s92\n"
      "s_cmp_lt_i32 s90, 0\n"
      "s_cbranch_scc1 .Lpd34_%=\n"
      ".Lpd35_%=:\n"
      "buffer_load_dwordx4 v[24:27], v117, s[20:23], s41 offen offset:128 nt\n"
      "v_perm_b32 v101, v40, v36, s47\n"
      "v_perm_b32 v103, v40, v36, s48\n"
      "v_perm_b32 v105, v41, v37, s47\n"
      "v_mfma_f32_16x16x16_bf16 v[44:47], v[100:101], v[90:91], v[44:47]\n"
      "v_perm_b32 v107, v41, v37, s48\n"
      "v_mfma_f32_16x16x16_bf16 v[48:51], v[102:103], v[90:91], v[48:51]\n"
      "v_perm_b32 v109, v42, v38, s47\n"
      "v_mfma_f32_16x16x16_bf16 v[52:55], v[104:105], v[90:91], v[52:55]\n"
      "v_perm_b32 v111, v42, v38, s48\n"
      "v_mfma_f32_16x16x16_bf16 v[56:59], v[106:107], v[90:91], v[56:59]\n"
      "v_perm_b32 v113, v43, v39, s47\n"
      "v_perm_b32 v115, v43, v39, s48\n"
      "v_mfma_f32_16x16x16_bf16 v[60:63], v[108:109], v[90:91], v[60:63]\n"
      "s_mov_b32 s40, s41\n"
      "v_mfma_f32_16x16x16_bf16 v[64:67], v[110:111], v[90:91], v[64:67]\n"
      "s_cmp_lt_u32 s42, s43\n"
      "v_mfma_f32_16x16x16_bf16 v[68:71], v[112:113], v[90:91], v[68:71]\n"
      "v_mfma_f32_16x16x16_bf16 v[72:75], v[114:115], v[90:91], v[72:75]\n"
      "s_cbranch_scc1 .Lpd4_%=\n"
      ".Lpd5_%=:\n"
      "s_add_i32 s93, s93, -1024\n"
      "s_max_i32 s93, s93, 0\n"
      "s_memrealtime s[90:91]\n"
      "s_add_u32 s92, s92, s93\n"
      "s_waitcnt lgkmcnt(0)\n"
      "s_lshl_b32 s90, s90, 8\n"
      "s_sub_i32 s90, s90, s92\n"
      "s_cmp_ge_i32 s90, s44\n"
      "s_waitcnt vmcnt(3)\n"
      "s_cbranch_scc1 .Lpd38_%=\n"
      "s_cmp_lt_i32 s90, 0\n"
      "s_waitcnt vmcnt(2)\n"
      "s_cbranch_scc0 .Lpd38_%=\n"
      ".Lpd37_%=:\n"
      "s_memrealtime s[90:91]\n"
      "s_waitcnt lgkmcnt(0)\n"
      "s_lshl_b32 s90, s90, 8\n"
      "s_sub_i32 s90, s90, s92\n"
      "s_cmp_lt_i32 s90, 0\n"
      "s_cbranch_scc1 .Lpd37_%=\n"
      ".Lpd38_%=:\n"
      "buffer_load_dwordx4 v[28:31], v118, s[24:27], s40 offen nt\n"
      "s_memrealtime s[90:91]\n"
      "s_add_u32 s92, s92, s93\n"
      "s_waitcnt lgkmcnt(0)\n"
      "s_lshl_b32 s90, s90, 8\n"
      "s_sub_i32 s90, s90, s92\n"
      "s_cmp_ge_i32 s90, s44\n"
      "s_waitcnt vmcnt(3)\n"
      "s_cbranch_scc1 .Lpd41_%=\n"
      "s_cmp_lt_i32 s90, 0\n"
      "s_waitcnt vmcnt(2)\n"
      "s_cbranch_scc0 .Lpd41_%=\n"
      ".Lpd40_%=:\n"
      "s_memrealtime s[90:91]\n"
      "s_waitcnt lgkmcnt(0)\n"
      "s_lshl_b32 s90, s90, 8\n"
      "s_sub_i32 s90, s90, s92\n"
      "s_cmp_lt_i32 s90, 0\n"
      "s_cbranch_scc1 .Lpd40_%=\n"
      ".Lpd41_%=:\n"
      "buffer_load_dwordx4 v[32:35], v118, s[24:27], s40 offen offset:1024 nt\n"
      "v_mfma_f32_16x16x32_bf16 v[76:79], v[12:15], v[4:7], 0\n"
      "v_mfma_f32_16x16x32_bf16 v[76:79], v[16:19], v[8:11], v[76:79]\n"
      "v_cmp_lt_i32_e64 s[60:61], 0, v120\n"
      "v_cmp_lt_i32_e64 s[62:63], 1, v120\n"
      "v_and_b32 v97, 7, v1\n"
      "s_lshl_b32 s52, s36, 2\n"
      "v_lshl_add_u32 v121, v97, 5, s52\n"
      "v_lshrrev_b32 v97, 5, v1\n"
      "v_lshl_add_u32 v97, v97, 2, v1\n"
      "v_and_b32 v97, 7, v97\n"
      "s_lshl_b32 s52, s36, 5\n"
      "v_lshl_add_u32 v124, v97, 2, s52\n"
      "s_mov_b64 s[58:59], 0xff\n"
      "s_memrealtime s[90:91]\n"
      "s_add_u32 s92, s92, s93\n"
      "s_waitcnt lgkmcnt(0)\n"
      "s_lshl_b32 s90, s90, 8\n"
      "s_sub_i32 s90, s90, s92\n"
      "s_cmp_ge_i32 s90, s44\n"
      "s_waitcnt vmcnt(3)\n"
      "s_cbranch_scc1 .Lpd44_%=\n"
      "s_cmp_lt_i32 s90, 0\n"
      "s_waitcnt vmcnt(2)\n"
      "s_cbranch_scc0 .Lpd44_%=\n"
      ".Lpd43_%=:\n"
      "s_memrealtime s[90:91]\n"
      "s_waitcnt lgkmcnt(0)\n"
      "s_lshl_b32 s90, s90, 8\n"
      "s_sub_i32 s90, s90, s92\n"
      "s_cmp_lt_i32 s90, 0\n"
      "s_cbranch_scc1 .Lpd43_%=\n"
      ".Lpd44_%=:\n"
      "buffer_load_dwordx4 v[36:39], v119, s[24:27], s40 offen nt\n"
      "s_memrealtime s[90:91]\n"
      "s_add_u32 s92, s92, s93\n"
      "s_waitcnt lgkmcnt(0)\n"
      "s_lshl_b32 s90, s90, 8\n"
      "s_sub_i32 s90, s90, s92\n"
      "s_cmp_ge_i32 s90, s44\n"
      "s_waitcnt vmcnt(3)\n"
      "s_cbranch_scc1 .Lpd47_%=\n"
      "s_cmp_lt_i32 s90, 0\n"
      "s_waitcnt vmcnt(2)\n"
      "s_cbranch_scc0 .Lpd47_%=\n"
      ".Lpd46_%=:\n"
      "s_memrealtime s[90:91]\n"
      "s_waitcnt lgkmcnt(0)\n"
      "s_lshl_b32 s90, s90, 8\n"
      "s_sub_i32 s90, s90, s92\n"
      "s_cmp_lt_i32 s90, 0\n"
      "s_cbranch_scc1 .Lpd46_%=\n"
      ".Lpd47_%=:\n"
      "buffer_load_dwordx4 v[40:43], v119, s[24:27], s40 offen offset:1024 nt\n"
      "v_mfma_f32_16x16x32_bf16 v[80:83], v[20:23], v[4:7], 0\n"
      "v_add_f32_dpp v84, v77, v76 row_ror:8 row_mask:0xf bank_mask:0xf\n"
      "v_mfma_f32_16x16x32_bf16 v[80:83], v[24:27], v[8:11], v[80:83]\n"
      "v_add_f32_dpp v85, v79, v78 row_ror:8 row_mask:0xf bank_mask:0xf\n"
      "v_mov_b32 v95, 0xf149f2ca\n"
      "v_cndmask_b32_e64 v84, v95, v84, s[60:61]\n"
      "v_cndmask_b32_e64 v85, v95, v85, s[62:63]\n"
      "v_cmp_lt_i32_e64 s[60:61], 8, v120\n"
      "v_cmp_lt_i32_e64 s[62:63], 9, v120\n"
      "v_max_f32 v94, v84, v85\n"
      "s_nop 0\n"
      "v_add_f32_dpp v86, v81, v80 row_ror:8 row_mask:0xf bank_mask:0xf\n"
      "v_add_f32_dpp v87, v83, v82 row_ror:8 row_mask:0xf bank_mask:0xf\n"
      "v_cndmask_b32_e64 v86, v95, v86, s[60:61]\n"
      "v_cndmask_b32_e64 v87, v95, v87, s[62:63]\n"
      "v_max3_f32 v94, v94, v86, v87\n"
      "v_mov_b32 v95, v94\n"
      "s_nop 1\n"
      "v_permlane16_swap_b32 v94, v95\n"
      "v_max_f32 v94, v94, v95\n"
      "v_mov_b32 v95, v94\n"
      "s_nop 1\n"
      "v_permlane32_swap_b32 v94, v95\n"
      "v_max_f32 v94, v94, v95\n"
      "v_mul_f32 v94, s46, v94\n"
      "v_max_f32 v96, v88, v94\n"
      "v_sub_f32 v92, v88, v96\n"
      "v_fma_f32 v84, v84, s46, -v96\n"
      "v_fma_f32 v85, v85, s46, -v96\n"
      "v_fma_f32 v86, v86, s46, -v96\n"
      "v_fma_f32 v87, v87, s46, -v96\n"
      "v_exp_f32 v92, v92\n"
      "v_exp_f32 v84, v84\n"
      "v_exp_f32 v85, v85\n"
      "v_exp_f32 v86, v86\n"
      "v_exp_f32 v87, v87\n"
      "v_mov_b32 v88, v96\n"
      "v_add_f32 v94, v84, v85\n"
      "v_add_f32 v95, v86, v87\n"
      "v_cvt_pk_bf16_f32 v90, v84, v85\n"
      "v_add_f32 v94, v94, v95\n"
      "v_cvt_pk_bf16_f32 v91, v86, v87\n"
      "v_fma_f32 v89, v89, v92, v94\n"
      "v_pk_mul_f32 v[44:45], v[44:45], v[92:93] op_sel_hi:[1,0]\n"
      "v_pk_mul_f32 v[46:47], v[46:47], v[92:93] op_sel_hi:[1,0]\n"
      "v_pk_mul_f32 v[48:49], v[48:49], v[92:93] op_sel_hi:[1,0]\n"
      "v_pk_mul_f32 v[50:51], v[50:51], v[92:93] op_sel_hi:[1,0]\n"
      "v_pk_mul_f32 v[52:53], v[52:53], v[92:93] op_sel_hi:[1,0]\n"
      "v_pk_mul_f32 v[54:55], v[54:55], v[92:93] op_sel_hi:[1,0]\n"
      "v_pk_mul_f32 v[56:57], v[56:57], v[92:93] op_sel_hi:[1,0]\n"
      "v_pk_mul_f32 v[58:59], v[58:59], v[92:93] op_sel_hi:[1,0]\n"
      "v_pk_mul_f32 v[60:61], v[60:61], v[92:93] op_sel_hi:[1,0]\n"
      "v_pk_mul_f32 v[62:63], v[62:63], v[92:93] op_sel_hi:[1,0]\n"
      "v_pk_mul_f32 v[64:65], v[64:65], v[92:93] op_sel_hi:[1,0]\n"
      "v_pk_mul_f32 v[66:67], v[66:67], v[92:93] op_sel_hi:[1,0]\n"
      "v_pk_mul_f32 v[68:69], v[68:69], v[92:93] op_sel_hi:[1,0]\n"
      "v_pk_mul_f32 v[70:71], v[70:71], v[92:93] op_sel_hi:[1,0]\n"
      "v_pk_mul_f32 v[72:73], v[72:73], v[92:93] op_sel_hi:[1,0]\n"
      "v_pk_mul_f32 v[74:75], v[74:75], v[92:93] op_sel_hi:[1,0]\n"
      "v_mov_b32 v95, v89\n"
      "s_nop 1\n"
      "v_permlane16_swap_b32 v89, v95\n"
      "v_add_f32 v89, v89, v95\n"
      "v_mov_b32 v95, v89\n"
      "s_nop 1\n"
      "v_permlane32_swap_b32 v89, v95\n"
      "v_add_f32 v89, v89, v95\n"
      "s_mov_b64 exec, s[58:59]\n"
      "ds_write2st64_b32 v121, v88, v89 offset0:132 offset1:133\n"
      "s_mov_b64 exec, -1\n"
      "s_waitcnt lgkmcnt(0)\n"
      "s_barrier\n"
      "ds_read_b32 v84, v124 offset:33792\n"
      "ds_read_b32 v77, v93 offset:34432\n"
      "v_and_b32 v94, 7, v1\n"
      "v_and_b32 v97, 48, v1\n"
      "s_mul_i32 s52, s36, 4224\n"
      "v_mul_u32_u24 v122, 528, v94\n"
      "v_add3_u32 v122, v122, v97, s52\n"
      "v_and_b32 v95, 8, v1\n"
      "v_lshl_add_u32 v122, v95, 5, v122\n"
      "v_lshrrev_b32 v96, 5, v1\n"
      "v_mul_u32_u24 v123, 16896, v96\n"
      "v_and_b32 v95, 31, v1\n"
      "v_lshl_add_u32 v123, v95, 4, v123\n"
      "s_mul_i32 s52, s36, 528\n"
      "v_add_u32 v123, s52, v123\n"
      "v_and_b32 v95, 3, v1\n"
      "s_lshl_b32 s52, s36, 10\n"
      "v_lshl_add_u32 v126, v95, 8, s52\n"
      "v_and_b32 v95, 28, v1\n"
      "v_lshl_add_u32 v126, v95, 3, v126\n"
      "v_lshl_add_u32 v126, v96, 4, v126\n"
      "s_lshl_b32 s52, s37, 5\n"
      "s_add_u32 s52, s52, s38\n"
      "s_mul_i32 s52, s52, 8320\n"
      "s_add_u32 s52, s52, 4096\n"
      "s_add_u32 s28, s16, s52\n"
      "s_addc_u32 s29, s17, 0\n"
      "s_and_b32 s29, s29, 0xffff\n"
      "s_mov_b32 s30, 8320\n"
      "s_mov_b32 s31, 0x20000\n"
      "s_waitcnt lgkmcnt(0)\n"
      "v_max_f32_dpp v85, v84, v84 quad_perm:[1,0,3,2] row_mask:0xf bank_mask:0xf\n"
      "v_mov_b32 v79, v77\n"
      "s_nop 0\n"
      "v_max_f32_dpp v85, v85, v85 quad_perm:[2,3,0,1] row_mask:0xf bank_mask:0xf\n"
      "s_nop 1\n"
      "v_max_f32_dpp v85, v85, v85 row_half_mirror row_mask:0xf bank_mask:0xf\n"
      "v_sub_f32 v84, v84, v85\n"
      "v_exp_f32 v84, v84\n"
      "s_waitcnt vmcnt(2)\n"
      "v_perm_b32 v100, v32, v28, s47\n"
      "v_perm_b32 v102, v32, v28, s48\n"
      "v_perm_b32 v104, v33, v29, s47\n"
      "v_perm_b32 v106, v33, v29, s48\n"
      "v_perm_b32 v108, v34, v30, s47\n"
      "v_perm_b32 v110, v34, v30, s48\n"
      "v_perm_b32 v112, v35, v31, s47\n"
      "v_perm_b32 v114, v35, v31, s48\n"
      "s_waitcnt vmcnt(0)\n"
      "v_perm_b32 v101, v40, v36, s47\n"
      "v_perm_b32 v109, v42, v38, s47\n"
      "v_perm_b32 v103, v40, v36, s48\n"
      "v_mfma_f32_16x16x16_bf16 v[44:47], v[100:101], v[90:91], v[44:47]\n"
      "v_perm_b32 v111, v42, v38, s48\n"
      "v_mfma_f32_16x16x16_bf16 v[60:63], v[108:109], v[90:91], v[60:63]\n"
      "v_perm_b32 v105, v41, v37, s47\n"
      "v_mfma_f32_16x16x16_bf16 v[48:51], v[102:103], v[90:91], v[48:51]\n"
      "v_perm_b32 v113, v43, v39, s47\n"
      "v_mfma_f32_16x16x16_bf16 v[64:67], v[110:111], v[90:91], v[64:67]\n"
      "v_perm_b32 v107, v41, v37, s48\n"
      "v_mfma_f32_16x16x16_bf16 v[52:55], v[104:105], v[90:91], v[52:55]\n"
      "v_perm_b32 v115, v43, v39, s48\n"
      "v_mfma_f32_16x16x16_bf16 v[68:71], v[112:113], v[90:91], v[68:71]\n"
      "v_mov_b32_dpp v44, v60 row_shr:8 row_mask:0xf bank_mask:0xc\n"
      "v_mov_b32_dpp v45, v61 row_shr:8 row_mask:0xf bank_mask:0xc\n"
      "v_mfma_f32_16x16x16_bf16 v[56:59], v[106:107], v[90:91], v[56:59]\n"
      "v_mov_b32_dpp v46, v62 row_shr:8 row_mask:0xf bank_mask:0xc\n"
      "v_mov_b32_dpp v47, v63 row_shr:8 row_mask:0xf bank_mask:0xc\n"
      "v_mfma_f32_16x16x16_bf16 v[72:75], v[114:115], v[90:91], v[72:75]\n"
      "ds_write_b128 v122, v[44:47] offset:0\n"
      "v_mov_b32_dpp v48, v64 row_shr:8 row_mask:0xf bank_mask:0xc\n"
      "v_mov_b32_dpp v49, v65 row_shr:8 row_mask:0xf bank_mask:0xc\n"
      "v_mov_b32_dpp v50, v66 row_shr:8 row_mask:0xf bank_mask:0xc\n"
      "v_mov_b32_dpp v51, v67 row_shr:8 row_mask:0xf bank_mask:0xc\n"
      "ds_write_b128 v122, v[48:51] offset:64\n"
      "v_mov_b32_dpp v52, v68 row_shr:8 row_mask:0xf bank_mask:0xc\n"
      "v_mov_b32_dpp v53, v69 row_shr:8 row_mask:0xf bank_mask:0xc\n"
      "v_mov_b32_dpp v54, v70 row_shr:8 row_mask:0xf bank_mask:0xc\n"
      "v_mov_b32_dpp v55, v71 row_shr:8 row_mask:0xf bank_mask:0xc\n"
      "ds_write_b128 v122, v[52:55] offset:128\n"
      "v_mov_b32_dpp v56, v72 row_shr:8 row_mask:0xf bank_mask:0xc\n"
      "v_mov_b32_dpp v57, v73 row_shr:8 row_mask:0xf bank_mask:0xc\n"
      "v_mov_b32_dpp v58, v74 row_shr:8 row_mask:0xf bank_mask:0xc\n"
      "v_mov_b32_dpp v59, v75 row_shr:8 row_mask:0xf bank_mask:0xc\n"
      "ds_write_b128 v122, v[56:59] offset:192\n"
      ".Lpd3_%=:\n"
      "s_waitcnt lgkmcnt(0)\n"
      "s_barrier\n"
      "ds_read_b128 v[12:15], v123 offset:0\n"
      "ds_read_b128 v[16:19], v123 offset:4224\n"
      "ds_read_b128 v[20:23], v123 offset:8448\n"
      "ds_read_b128 v[24:27], v123 offset:12672\n"
      "s_waitcnt lgkmcnt(3)\n"
      "v_mul_f32_dpp v28, v84, v12 row_newbcast:0 row_mask:0xf bank_mask:0xf\n"
      "v_mul_f32_dpp v30, v84, v14 row_newbcast:0 row_mask:0xf bank_mask:0xf\n"
      "v_mul_f32_dpp v29, v84, v13 row_newbcast:0 row_mask:0xf bank_mask:0xf\n"
      "v_mul_f32_dpp v31, v84, v15 row_newbcast:0 row_mask:0xf bank_mask:0xf\n"
      "s_waitcnt lgkmcnt(2)\n"
      "v_fmac_f32_dpp v28, v84, v16 row_newbcast:1 row_mask:0xf bank_mask:0xf\n"
      "v_fmac_f32_dpp v30, v84, v18 row_newbcast:1 row_mask:0xf bank_mask:0xf\n"
      "v_fmac_f32_dpp v29, v84, v17 row_newbcast:1 row_mask:0xf bank_mask:0xf\n"
      "v_fmac_f32_dpp v31, v84, v19 row_newbcast:1 row_mask:0xf bank_mask:0xf\n"
      "s_waitcnt lgkmcnt(1)\n"
      "v_fmac_f32_dpp v28, v84, v20 row_newbcast:2 row_mask:0xf bank_mask:0xf\n"
      "v_fmac_f32_dpp v30, v84, v22 row_newbcast:2 row_mask:0xf bank_mask:0xf\n"
      "v_fmac_f32_dpp v29, v84, v21 row_newbcast:2 row_mask:0xf bank_mask:0xf\n"
      "v_fmac_f32_dpp v31, v84, v23 row_newbcast:2 row_mask:0xf bank_mask:0xf\n"
      "s_waitcnt lgkmcnt(0)\n"
      "v_fmac_f32_dpp v28, v84, v24 row_newbcast:3 row_mask:0xf bank_mask:0xf\n"
      "v_fmac_f32_dpp v30, v84, v26 row_newbcast:3 row_mask:0xf bank_mask:0xf\n"
      "v_fmac_f32_dpp v29, v84, v25 row_newbcast:3 row_mask:0xf bank_mask:0xf\n"
      "v_fmac_f32_dpp v31, v84, v27 row_newbcast:3 row_mask:0xf bank_mask:0xf\n"
      "v_permlane32_swap_b32 v28, v30\n"
      "s_nop 0\n"
      "v_permlane32_swap_b32 v29, v31\n"
      "v_add_f32 v76, v28, v30\n"
      "v_add_f32 v78, v29, v31\n"
      "buffer_store_dwordx4 v[76:79], v126, s[28:31], 0 offen sc1\n"
      "s_branch .Lpd6_%=\n"
      ".Lpd2_%=:\n"
      "v_and_b32 v97, 7, v1\n"
      "s_lshl_b32 s52, s36, 2\n"
      "v_lshl_add_u32 v121, v97, 5, s52\n"
      "v_lshrrev_b32 v97, 5, v1\n"
      "v_lshl_add_u32 v97, v97, 2, v1\n"
      "v_and_b32 v97, 7, v97\n"
      "s_lshl_b32 s52, s36, 5\n"
      "v_lshl_add_u32 v124, v97, 2, s52\n"
      "s_mov_b64 s[58:59], 0xff\n"
      "s_mov_b64 exec, s[58:59]\n"
      "ds_write2st64_b32 v121, v88, v89 offset0:132 offset1:133\n"
      "s_mov_b64 exec, -1\n"
      "s_waitcnt vmcnt(0)\n"
      "s_waitcnt lgkmcnt(0)\n"
      "s_barrier\n"
      "ds_read_b32 v84, v124 offset:33792\n"
      "ds_read_b32 v77, v93 offset:34432\n"
      "v_and_b32 v94, 7, v1\n"
      "v_and_b32 v97, 48, v1\n"
      "s_mul_i32 s52, s36, 4224\n"
      "v_mul_u32_u24 v122, 528, v94\n"
      "v_add3_u32 v122, v122, v97, s52\n"
      "v_and_b32 v95, 8, v1\n"
      "v_lshl_add_u32 v122, v95, 5, v122\n"
      "v_lshrrev_b32 v96, 5, v1\n"
      "v_mul_u32_u24 v123, 16896, v96\n"
      "v_and_b32 v95, 31, v1\n"
      "v_lshl_add_u32 v123, v95, 4, v123\n"
      "s_mul_i32 s52, s36, 528\n"
      "v_add_u32 v123, s52, v123\n"
      "v_and_b32 v95, 3, v1\n"
      "s_lshl_b32 s52, s36, 10\n"
      "v_lshl_add_u32 v126, v95, 8, s52\n"
      "v_and_b32 v95, 28, v1\n"
      "v_lshl_add_u32 v126, v95, 3, v126\n"
      "v_lshl_add_u32 v126, v96, 4, v126\n"
      "s_lshl_b32 s52, s37, 5\n"
      "s_add_u32 s52, s52, s38\n"
      "s_mul_i32 s52, s52, 8320\n"
      "s_add_u32 s52, s52, 4096\n"
      "s_add_u32 s28, s16, s52\n"
      "s_addc_u32 s29, s17, 0\n"
      "s_and_b32 s29, s29, 0xffff\n"
      "s_mov_b32 s30, 8320\n"
      "s_mov_b32 s31, 0x20000\n"
      "s_waitcnt lgkmcnt(0)\n"
      "v_max_f32_dpp v85, v84, v84 quad_perm:[1,0,3,2] row_mask:0xf bank_mask:0xf\n"
      "v_mov_b32 v79, v77\n"
      "s_nop 0\n"
      "v_max_f32_dpp v85, v85, v85 quad_perm:[2,3,0,1] row_mask:0xf bank_mask:0xf\n"
      "s_nop 1\n"
      "v_max_f32_dpp v85, v85, v85 row_half_mirror row_mask:0xf bank_mask:0xf\n"
      "v_sub_f32 v84, v84, v85\n"
      "v_exp_f32 v84, v84\n"
      "ds_write_b128 v122, v[44:47] offset:0\n"
      "ds_write_b128 v122, v[48:51] offset:64\n"
      "ds_write_b128 v122, v[52:55] offset:128\n"
      "ds_write_b128 v122, v[56:59] offset:192\n"
      "s_waitcnt vmcnt(0)\n"
      "s_branch .Lpd3_%=\n"
      ".Lpd1_%=:\n"
      "v_lshrrev_b32 v4, 4, v1\n"
      "v_and_b32 v5, 15, v1\n"
      "s_mul_i32 s52, s37, 266240\n"
      "s_add_u32 s52, s52, 4096\n"
      "s_mul_i32 s53, s38, 8320\n"
      "s_lshl_b32 s54, s37, 11\n"
      "s_lshl_b32 s55, s38, 6\n"
      "s_add_u32 s54, s54, s55\n"
      "s_lshl_b32 s55, s37, 8\n"
      "v_lshlrev_b32 v49, 7, v1\n"
      "v_and_b32 v9, 31, v1\n"
      "v_lshl_add_u32 v9, v9, 2, s55\n"
      "s_mov_b32 s30, 266240\n"
      "s_mov_b32 s31, 0x20000\n"
      "s_mov_b32 s22, 64\n"
      "s_mov_b32 s23, 0x20000\n"
      "s_lshl_b64 s[60:61], 1, s38\n"
      "s_waitcnt lgkmcnt(0)\n"
      "s_load_dword s45, s[16:17], s55\n"
      "s_load_dwordx2 s[50:51], s[12:13], 0x0\n"
      "s_add_u32 s28, s16, s52\n"
      "s_addc_u32 s29, s17, 0\n"
      "s_and_b32 s29, s29, 0xffff\n"
      "s_add_u32 s20, s14, s54\n"
      "s_addc_u32 s21, s15, 0\n"
      "s_and_b32 s21, s21, 0xffff\n"
      "s_add_u32 s42, s53, 8192\n"
      "s_mov_b64 exec, 1\n"
      "s_mov_b64 exec, -1\n"
      "v_and_b32 v6, 7, v1\n"
      "v_lshl_add_u32 v6, v6, 2, v4\n"
      "v_mul_u32_u24 v6, 8320, v6\n"
      "s_lshr_b32 s52, s38, 2\n"
      "s_lshl_b32 s52, s52, 4\n"
      "s_add_u32 s52, s52, 8192\n"
      "v_add_u32 v6, s52, v6\n"
      "v_mul_u32_u24 v7, 8320, v4\n"
      "v_lshl_add_u32 v7, v5, 4, v7\n"
      "s_lshl_b32 s52, s38, 8\n"
      "v_add_u32 v7, s52, v7\n"
      "v_and_b32 v8, 1, v1\n"
      "v_lshlrev_b32 v8, 5, v8\n"
      "v_lshrrev_b32 v53, 5, v1\n"
      "v_lshl_add_u32 v8, v53, 4, v8\n"
      "v_and_b32 v53, 12, v1\n"
      "v_add_u32 v8, v8, v53\n"
      "s_mov_b32 s56, 0x3333\n"
      "s_mov_b32 s57, 0x3333\n"
      "s_mov_b32 s39, 33280\n"
      "s_mov_b32 s40, 66560\n"
      "s_mov_b32 s41, 99840\n"
      "s_mov_b32 s42, 133120\n"
      "s_mov_b32 s43, 166400\n"
      "s_mov_b32 s44, 199680\n"
      "s_mov_b32 s46, 232960\n"
      "s_waitcnt lgkmcnt(0) vmcnt(0)\n"
      "s_add_u32 s45, s45, 1\n"
      "s_cmp_gt_i32 s50, 0\n"
      "s_cselect_b32 s50, s50, 0x8000\n"
      "s_min_i32 s50, s50, 0x8000\n"
      "s_cmp_gt_i32 s51, 0\n"
      "s_cselect_b32 s51, s51, 0x8000\n"
      "s_min_i32 s51, s51, 0x8000\n"
      "s_add_u32 s54, s50, s51\n"
      "v_cvt_f32_u32 v53, s54\n"
      "v_mul_f32 v53, 0x40cfff05, v53\n"
      "v_cvt_u32_f32 v53, v53\n"
      "s_add_u32 s92, s88, 0\n"
      "s_lshl_b32 s92, s92, 8\n"
      "v_readfirstlane_b32 s49, v53\n"
      "s_mov_b32 s47, 0\n"
      "s_add_u32 s49, s49, s92\n"
      "v_lshlrev_b32 v49, 2, v1\n"
      "v_lshlrev_b32 v48, 1, v1\n"
      "v_add_u32 v48, 8192, v48\n"
      "v_mov_b32 v73, s45\n"
      "v_mov_b32 v75, s45\n"
      "s_mov_b32 s62, 0x01010101\n"
      "s_mov_b32 s63, 0x01010101\n"
      "ds_write_b32 v93, v73 offset:34432\n"
      "s_waitcnt lgkmcnt(0)\n"
      "s_barrier\n"
      "ds_read2st64_b32 v[50:51], v49 offset0:132 offset1:133\n"
      "s_waitcnt lgkmcnt(0)\n"
      "v_max_f32_dpp v72, v50, v50 quad_perm:[1,0,3,2] row_mask:0xf bank_mask:0xf\n"
      "s_nop 1\n"
      "v_max_f32_dpp v72, v72, v72 quad_perm:[2,3,0,1] row_mask:0xf bank_mask:0xf\n"
      "s_nop 1\n"
      "v_max_f32_dpp v72, v72, v72 row_half_mirror row_mask:0xf bank_mask:0xf\n"
      "v_sub_f32 v50, v50, v72\n"
      "v_exp_f32 v50, v50\n"
      "s_nop 0\n"
      "v_mul_f32 v74, v50, v51\n"
      "s_nop 1\n"
      "v_add_f32_dpp v74, v74, v74 quad_perm:[1,0,3,2] row_mask:0xf bank_mask:0xf\n"
      "s_nop 1\n"
      "v_add_f32_dpp v74, v74, v74 quad_perm:[2,3,0,1] row_mask:0xf bank_mask:0xf\n"
      "s_nop 1\n"
      "v_add_f32_dpp v74, v74, v74 row_half_mirror row_mask:0xf bank_mask:0xf\n"
      "s_mul_i32 s53, s38, 8320\n"
      "s_mov_b64 exec, s[62:63]\n"
      "buffer_store_dwordx4 v[72:75], v48, s[28:31], s53 offen sc1\n"
      "s_mov_b64 exec, -1\n"
      "s_add_u32 s49, s49, 0\n"
      "s_waitcnt lgkmcnt(0)\n"
      "s_barrier\n"
      "s_memrealtime s[90:91]\n"
      "s_waitcnt lgkmcnt(0)\n"
      "s_add_u32 s90, s90, 64\n"
      "s_lshl_b32 s90, s90, 8\n"
      "s_sub_i32 s52, s49, s90\n"
      "s_cmp_lt_u32 s52, 51200\n"
      "s_cselect_b32 s49, s49, s90\n"
      "s_sub_u32 s92, s49, 10752\n"
      ".Lpd50_%=:\n"
      "s_memrealtime s[90:91]\n"
      "s_waitcnt lgkmcnt(0)\n"
      "s_lshl_b32 s90, s90, 8\n"
      "s_sub_i32 s90, s90, s92\n"
      "s_cmp_lt_i32 s90, 0\n"
      "s_cbranch_scc1 .Lpd50_%=\n"
      "buffer_load_dwordx4 v[12:15], v6, s[28:31], 0 offen sc1\n"
      ".Lpd51_%=:\n"
      "s_memrealtime s[90:91]\n"
      "s_waitcnt lgkmcnt(0)\n"
      "s_lshl_b32 s90, s90, 8\n"
      "s_sub_i32 s90, s90, s49\n"
      "s_cmp_lt_i32 s90, 0\n"
      "s_cbranch_scc1 .Lpd51_%=\n"
      "buffer_load_dwordx4 v[16:19], v7, s[28:31], 0 offen sc1\n"
      "buffer_load_dwordx4 v[20:23], v7, s[28:31], s39 offen sc1\n"
      "buffer_load_dwordx4 v[24:27], v7, s[28:31], s40 offen sc1\n"
      "buffer_load_dwordx4 v[28:31], v7, s[28:31], s41 offen sc1\n"
      "buffer_load_dwordx4 v[32:35], v7, s[28:31], s42 offen sc1\n"
      "buffer_load_dwordx4 v[36:39], v7, s[28:31], s43 offen sc1\n"
      "buffer_load_dwordx4 v[40:43], v7, s[28:31], s44 offen sc1\n"
      "buffer_load_dwordx4 v[44:47], v7, s[28:31], s46 offen sc1\n"
      ".Lpd48_%=:\n"
      "s_waitcnt vmcnt(8)\n"
      "v_max_f32_dpp v49, v12, v12 quad_perm:[1,0,3,2] row_mask:0xf bank_mask:0xf\n"
      "v_min_u32 v51, v13, v15\n"
      "v_max_u32 v52, v13, v15\n"
      "v_max_f32_dpp v49, v49, v49 quad_perm:[2,3,0,1] row_mask:0xf bank_mask:0xf\n"
      "s_nop 1\n"
      "v_max_f32_dpp v49, v49, v49 row_half_mirror row_mask:0xf bank_mask:0xf\n"
      "v_mov_b32 v50, v49\n"
      "s_nop 1\n"
      "v_permlane16_swap_b32 v49, v50\n"
      "v_max_f32 v49, v49, v50\n"
      "v_mov_b32 v50, v49\n"
      "s_nop 1\n"
      "v_permlane32_swap_b32 v49, v50\n"
      "v_max_f32 v49, v49, v50\n"
      "v_sub_f32 v48, v12, v49\n"
      "v_exp_f32 v48, v48\n"
      "s_nop 0\n"
      "v_mul_f32 v55, v48, v14\n"
      "s_waitcnt vmcnt(7)\n"
      "v_mul_f32_dpp v53, v48, v16 row_newbcast:0 row_mask:0xf bank_mask:0xf\n"
      "v_mul_f32_dpp v54, v48, v18 row_newbcast:0 row_mask:0xf bank_mask:0xf\n"
      "v_min3_u32 v51, v51, v17, v19\n"
      "v_max3_u32 v52, v52, v17, v19\n"
      "v_add_f32_dpp v55, v55, v55 quad_perm:[1,0,3,2] row_mask:0xf bank_mask:0xf\n"
      "s_waitcnt vmcnt(6)\n"
      "v_fmac_f32_dpp v53, v48, v20 row_newbcast:1 row_mask:0xf bank_mask:0xf\n"
      "v_fmac_f32_dpp v54, v48, v22 row_newbcast:1 row_mask:0xf bank_mask:0xf\n"
      "v_min3_u32 v51, v51, v21, v23\n"
      "v_max3_u32 v52, v52, v21, v23\n"
      "v_add_f32_dpp v55, v55, v55 quad_perm:[2,3,0,1] row_mask:0xf bank_mask:0xf\n"
      "s_waitcnt vmcnt(5)\n"
      "v_fmac_f32_dpp v53, v48, v24 row_newbcast:2 row_mask:0xf bank_mask:0xf\n"
      "v_fmac_f32_dpp v54, v48, v26 row_newbcast:2 row_mask:0xf bank_mask:0xf\n"
      "v_min3_u32 v51, v51, v25, v27\n"
      "v_max3_u32 v52, v52, v25, v27\n"
      "v_add_f32_dpp v55, v55, v55 row_half_mirror row_mask:0xf bank_mask:0xf\n"
      "v_mov_b32 v50, v55\n"
      "s_waitcnt vmcnt(4)\n"
      "v_fmac_f32_dpp v53, v48, v28 row_newbcast:3 row_mask:0xf bank_mask:0xf\n"
      "v_fmac_f32_dpp v54, v48, v30 row_newbcast:3 row_mask:0xf bank_mask:0xf\n"
      "v_min3_u32 v51, v51, v29, v31\n"
      "v_max3_u32 v52, v52, v29, v31\n"
      "v_permlane16_swap_b32 v55, v50\n"
      "v_add_f32 v55, v55, v50\n"
      "v_mov_b32 v50, v55\n"
      "s_waitcnt vmcnt(3)\n"
      "v_fmac_f32_dpp v53, v48, v32 row_newbcast:4 row_mask:0xf bank_mask:0xf\n"
      "v_fmac_f32_dpp v54, v48, v34 row_newbcast:4 row_mask:0xf bank_mask:0xf\n"
      "v_min3_u32 v51, v51, v33, v35\n"
      "v_max3_u32 v52, v52, v33, v35\n"
      "v_permlane32_swap_b32 v55, v50\n"
      "v_add_f32 v55, v55, v50\n"
      "s_waitcnt vmcnt(2)\n"
      "v_fmac_f32_dpp v53, v48, v36 row_newbcast:5 row_mask:0xf bank_mask:0xf\n"
      "v_fmac_f32_dpp v54, v48, v38 row_newbcast:5 row_mask:0xf bank_mask:0xf\n"
      "v_min3_u32 v51, v51, v37, v39\n"
      "v_max3_u32 v52, v52, v37, v39\n"
      "v_rcp_f32 v55, v55\n"
      "s_waitcnt vmcnt(1)\n"
      "v_fmac_f32_dpp v53, v48, v40 row_newbcast:6 row_mask:0xf bank_mask:0xf\n"
      "v_fmac_f32_dpp v54, v48, v42 row_newbcast:6 row_mask:0xf bank_mask:0xf\n"
      "v_min3_u32 v51, v51, v41, v43\n"
      "v_max3_u32 v52, v52, v41, v43\n"
      "s_waitcnt vmcnt(0)\n"
      "v_fmac_f32_dpp v53, v48, v44 row_newbcast:7 row_mask:0xf bank_mask:0xf\n"
      "v_fmac_f32_dpp v54, v48, v46 row_newbcast:7 row_mask:0xf bank_mask:0xf\n"
      "v_min3_u32 v51, v51, v45, v47\n"
      "v_max3_u32 v52, v52, v45, v47\n"
      "v_cmp_ne_u32 vcc, s45, v51\n"
      "v_cmp_ne_u32_e64 s[60:61], s45, v52\n"
      "v_permlane32_swap_b32 v53, v54\n"
      "v_add_f32 v53, v53, v54\n"
      "v_mov_b32 v54, v53\n"
      "s_or_b64 s[60:61], s[60:61], vcc\n"
      "s_cbranch_scc0 .Lpd49_%=\n"
      "buffer_load_dwordx4 v[12:15], v6, s[28:31], 0 offen sc1\n"
      "buffer_load_dwordx4 v[16:19], v7, s[28:31], 0 offen sc1\n"
      "buffer_load_dwordx4 v[20:23], v7, s[28:31], s39 offen sc1\n"
      "buffer_load_dwordx4 v[24:27], v7, s[28:31], s40 offen sc1\n"
      "buffer_load_dwordx4 v[28:31], v7, s[28:31], s41 offen sc1\n"
      "buffer_load_dwordx4 v[32:35], v7, s[28:31], s42 offen sc1\n"
      "buffer_load_dwordx4 v[36:39], v7, s[28:31], s43 offen sc1\n"
      "buffer_load_dwordx4 v[40:43], v7, s[28:31], s44 offen sc1\n"
      "buffer_load_dwordx4 v[44:47], v7, s[28:31], s46 offen sc1\n"
      "s_add_u32 s47, s47, 1\n"
      "s_cmp_lt_u32 s47, 0x100000\n"
      "s_cbranch_scc1 .Lpd48_%=\n"
      ".Lpd49_%=:\n"
      "v_permlane16_swap_b32 v53, v54\n"
      "v_add_f32 v53, v53, v54\n"
      "v_mul_f32 v53, v53, v55\n"
      "s_cmp_eq_u32 s38, 0\n"
      "v_mov_b32 v50, s45\n"
      "v_mov_b32_dpp v54, v53 quad_perm:[2,3,0,1] row_mask:0xf bank_mask:0xf\n"
      "v_cvt_pk_bf16_f32 v53, v53, v54\n"
      "s_mov_b64 exec, s[56:57]\n"
      "buffer_store_dword v53, v8, s[20:23], 0 offen sc1\n"
      "s_cbranch_scc0 .Lpd6_%=\n"
      "s_mov_b64 exec, 1\n"
      "v_mov_b32 v49, s55\n"
      "global_store_dword v49, v50, s[16:17] sc1\n"
      ".Lpd6_%=:\n"
      :
      : "s"(__builtin_amdgcn_kernarg_segment_ptr()), "s"(blockIdx.x), "v"(threadIdx.x), "s"((unsigned)(uintptr_t)(__attribute__((address_space(3))) float*)lds)
      : "v1", "v2", "v3", "v4", "v5", "v6", "v7", "v8", "v9", "v10", "v11", "v12", "v13", "v14", "v15", "v16", "v17", "v18", "v19", "v20", "v21", "v22",
        "v23", "v24", "v25", "v26", "v27", "v28", "v29", "v30", "v31", "v32", "v33", "v34", "v35", "v36", "v37", "v38", "v39", "v40", "v41", "v42", "v43",
        "v44", "v45", "v46", "v47", "v48", "v49", "v50", "v51", "v52", "v53", "v54", "v55", "v56", "v57", "v58", "v59", "v60", "v61", "v62", "v63", "v64",
        "v65", "v66", "v67", "v68", "v69", "v70", "v71", "v72", "v73", "v74", "v75", "v76", "v77", "v78", "v79", "v80", "v81", "v82", "v83", "v84", "v85",
        "v86", "v87", "v88", "v89", "v90", "v91", "v92", "v93", "v94", "v95", "v96", "v97", "v98", "v99", "v100", "v101", "v102", "v103", "v104", "v105",
        "v106", "v107", "v108", "v109", "v110", "v111", "v112", "v113", "v114", "v115", "v116", "v117", "v118", "v119", "v120", "v121", "v122", "v123",
        "v124", "v125", "v126", "v127", "v128", "v129", "v130", "v131", "v132", "v133", "s4", "s5", "s6", "s7", "s8", "s9", "s10", "s11", "s12", "s13", "s14",
        "s15", "s16", "s17", "s18", "s19", "s20", "s21", "s22", "s23", "s24", "s25", "s26", "s27", "s28", "s29", "s30", "s31", "s36", "s37", "s38", "s39",
        "s40", "s41", "s42", "s43", "s44", "s45", "s46", "s47", "s48", "s49", "s50", "s51", "s52", "s53", "s54", "s55", "s56", "s57", "s58", "s59", "s60",
        "s61", "s62", "s63", "s64", "s65", "s66", "s67", "s68", "s69", "s70", "s71", "s72", "s73", "s74", "s75", "s76", "s77", "s78", "s79", "s80", "s81",
        "s82", "s83", "s84", "s85", "s86", "s87", "s88", "s89", "s90", "s91", "s92", "s93", "vcc", "scc", "memory");
}

}

extern "C" void kernel_launch(void* const* d_in, const int* in_sizes, int n_in, void* d_out, int out_size, void* d_ws, size_t ws_size,
                              hipStream_t stream) {
  paged_decode_kernel<<<NWG, NT, 0, stream>>>(d_in[0], d_in[1], d_in[2], (const int*)d_in[3], (const int*)d_in[4], d_out, d_ws, in_sizes[1] * 2, 0);
}
```
